```python
import torch

def _softmax(x, axis=-1):
    return torch.softmax(x, dim=axis)
def _gelu(x):
    return torch.nn.functional.gelu(x, approximate="tanh")
def _silu(x):
    return torch.nn.functional.silu(x)
def _sigmoid(x):
    return torch.sigmoid(x)
def _layernorm(x, axis=-1, eps=1e-5):
    m = x.mean(dim=axis, keepdim=True)
    v = x.var(dim=axis, keepdim=True, unbiased=False)
    return (x - m) / torch.sqrt(v + eps)
def _scatter_add(tgt, idx, src, axis=0):
    return tgt.clone().index_add_(axis, idx, src)
def _segment_sum(src, seg, n):
    out = torch.zeros((n,) + src.shape[1:], dtype=src.dtype)
    return out.index_add_(0, seg, src)
def _pad(x, pw):
    flat = tuple(v for pair in reversed(pw) for v in pair)
    return torch.nn.functional.pad(x, flat)

kernel_name = 'concat_heads__linear_attn_block__ssm_conv_s4d__prompt'

def setup_inputs():

    _w = (lambda a: (lambda t: t.to(torch.bfloat16) if t.is_floating_point() else t)(torch.as_tensor(a)))
    def _A_setup():
        B, S, D = 1, 2048, 448
        return {
            "x": _w((lambda *s: torch.randn(*s, dtype=torch.bfloat16) * 0.05)(B, S, D)),
            "wqkv": _w((lambda *s: torch.randn(*s, dtype=torch.bfloat16) * 0.8)(D, 3 * D)),
            "wo": _w((lambda *s: torch.randn(*s, dtype=torch.bfloat16) * 0.05)(D, D)),
        }
    _A_in = _A_setup()

    def _B_setup():
        Bsz, L, D, N = 1, 2048, 448, 256
        return {
            "u": _w((lambda *s: torch.randn(*s, dtype=torch.bfloat16) * 0.05)(Bsz, L, D)),
            "log_A_real": _w((lambda *s: torch.randn(*s, dtype=torch.bfloat16) * 3.2)(D, N) * 0.5),
            "A_imag": _w((lambda *s: torch.randn(*s, dtype=torch.bfloat16) * 0.05)(D, N) * 3.14),
            "C_real": _w((lambda *s: torch.randn(*s, dtype=torch.bfloat16) * 0.2)(D, N) / N ** 0.5),
            "C_imag": _w((lambda *s: torch.randn(*s, dtype=torch.bfloat16) * 0.4)(D, N) / N ** 0.5),
            "log_dt": _w((lambda *s: torch.randn(*s, dtype=torch.bfloat16) * 0.05)(D) * 0.5 - 4.0),
            "D_skip": _w((lambda *s: torch.randn(*s, dtype=torch.bfloat16) * 0.05)(D)),
        }
    _B_in = _B_setup()

    return {
        "A_x": _A_in["x"],
        "A_wqkv": _A_in["wqkv"],
        "A_wo": _A_in["wo"],
        "B_u": _B_in["u"],
        "B_log_A_real": _B_in["log_A_real"],
        "B_A_imag": _B_in["A_imag"],
        "B_C_real": _B_in["C_real"],
        "B_C_imag": _B_in["C_imag"],
        "B_log_dt": _B_in["log_dt"],
        "B_D_skip": _B_in["D_skip"],
    }

def reference(A_x, A_wqkv, A_wo, B_u, B_log_A_real, B_A_imag, B_C_real, B_C_imag, B_log_dt, B_D_skip):
    def _A_ref(x, wqkv, wo):
        B, S, D = x.shape
        H = 16; Dh = D // H
        qkv = (lambda x, *d: x.permute(*d))((x @ wqkv).reshape(B, S, 3, H, Dh), 2, 0, 3, 1, 4)
        q, k, v = qkv[0], qkv[1], qkv[2]
        phi = lambda t: torch.where(t > 0, t + 1, torch.exp(t)) + 1e-6
        pq, pk = phi(q), phi(k)
        kv = torch.einsum("bhsd,bhse->bhde", pk, v)
        z = torch.einsum("bhsd->bhd", pk)
        num = torch.einsum("bhqd,bhde->bhqe", pq, kv)
        den = torch.einsum("bhqd,bhd->bhq", pq, z)[..., None] + 1e-6
        o = num / den
        return (lambda x, *d: x.permute(*d))(o, 0, 2, 1, 3).reshape(B, S, D) @ wo

    def _B_ref(u, log_A_real, A_imag, C_real, C_imag, log_dt, D_skip):
        L = u.shape[1]
        dt = torch.exp(log_dt)
        A = -torch.exp(log_A_real) + 1j * A_imag
        C = (C_real + 1j * C_imag) * dt[:, None]
        pos = (lambda x, dt: x.to(dt))(torch.arange(L), log_dt.dtype)
        P = torch.exp((A * dt[:, None])[:, :, None] * pos[None, None, :])
        K = 2.0 * torch.einsum("dn,dnl->dl", C, P).real
        L2 = 2 * L
        u_t = (lambda x, *d: x.permute(*d))(u, 0, 2, 1)
        Kf = (lambda x, *a, **k: torch.fft.rfft(x.float(), *a, **k))(K, L2)
        Uf = (lambda x, *a, **k: torch.fft.rfft(x.float(), *a, **k))(u_t, L2)
        y = (lambda x, *a, **k: torch.fft.irfft(x, *a, **k).to(torch.bfloat16))(Uf * Kf[None, :, :], L2)[:, :, :L]
        y = (lambda x, *d: x.permute(*d))(y, 0, 2, 1)
        return y + u * D_skip[None, None, :]

    a = _A_ref(A_x, A_wqkv, A_wo)
    b = _B_ref(B_u, B_log_A_real, B_A_imag, B_C_real, B_C_imag, B_log_dt, B_D_skip)
    return torch.concatenate([a, b], axis=-1)
```

```cpp
#include <hip/hip_runtime.h>
#include <hip/hip_bf16.h>

typedef unsigned short u16;
__device__ __forceinline__ float bf2f(u16 v) { return __uint_as_float(((unsigned)v) << 16); }
__device__ __forceinline__ u16 f2bf(float f) { __hip_bfloat16 b = __float2bfloat16(f); return *(u16*)&b; }
__device__ __forceinline__ float rbf(float f) { return bf2f(f2bf(f)); }

constexpr int S = 2048, D = 448, H = 16, DH = 28, N = 256;

__global__ void k_qkv(const u16* x, const u16* w, u16* qkv) {
    int col = blockIdx.x * 64 + (threadIdx.x & 63);
    int row = blockIdx.y * 4 + (threadIdx.x >> 6);
    if (col >= 3 * D) return;
    float acc = 0.f;
    for (int k = 0; k < D; ++k) acc += bf2f(x[row * D + k]) * bf2f(w[k * 3 * D + col]);
    float t = rbf(acc);
    if (col < 2 * D) {
        float p = t > 0.f ? rbf(t + 1.f) : rbf(expf(t));
        t = rbf(p + 1e-6f);
    }
    qkv[row * 3 * D + col] = f2bf(t);
}
__global__ void k_kv(const u16* qkv, float* kv) {
    int h = blockIdx.x, d = blockIdx.y;
    int e = threadIdx.x & 31, part = threadIdx.x >> 5;
    __shared__ float red[8][32];
    float acc = 0.f;
    if (e <= DH) {
        for (int s = part; s < S; s += 8) {
            float pk = bf2f(qkv[s * 3 * D + D + h * DH + d]);
            float v = e < DH ? bf2f(qkv[s * 3 * D + 2 * D + h * DH + e]) : 1.f;
            acc += pk * v;
        }
    }
    red[part][e] = acc;
    __syncthreads();
    if (part == 0 && e <= DH) {
        float a = 0.f;
        for (int p = 0; p < 8; ++p) a += red[p][e];
        kv[(h * DH + d) * 32 + e] = rbf(a);
    }
}
__global__ void k_o(const u16* qkv, const float* kv, u16* o) {
    int s = blockIdx.x;
    int c = threadIdx.x;
    if (c >= D) return;
    int h = c / DH, e = c % DH;
    float num = 0.f, den = 0.f;
    for (int d = 0; d < DH; ++d) {
        float pq = bf2f(qkv[s * 3 * D + h * DH + d]);
        num += pq * kv[(h * DH + d) * 32 + e];
        den += pq * kv[(h * DH + d) * 32 + DH];
    }
    num = rbf(num);
    den = rbf(rbf(den) + 1e-6f);
    o[s * D + c] = f2bf(num / den);
}
__global__ void k_out(const u16* o, const u16* wo, u16* out) {
    int col = blockIdx.x * 64 + (threadIdx.x & 63);
    int row = blockIdx.y * 4 + (threadIdx.x >> 6);
    float acc = 0.f;
    for (int k = 0; k < D; ++k) acc += bf2f(o[row * D + k]) * bf2f(wo[k * D + col]);
    out[row * 2 * D + col] = f2bf(acc);
}
__global__ void k_gen(const u16* lar, const u16* aim, const u16* cre, const u16* cim, const u16* ldt, float* Kf) {
    int d = blockIdx.x;
    int l = blockIdx.y * 256 + threadIdx.x;
    float dt = rbf(expf(bf2f(ldt[d])));
    float pos = (float)l;
    float acc = 0.f;
    for (int n = 0; n < N; ++n) {
        float ar = -rbf(expf(bf2f(lar[d * N + n]))) * dt;
        float ai = bf2f(aim[d * N + n]) * dt;
        float cr = bf2f(cre[d * N + n]) * dt, ci = bf2f(cim[d * N + n]) * dt;
        float m = expf(ar * pos);
        float sn, cs;
        sincosf(ai * pos, &sn, &cs);
        acc += cr * (m * cs) - ci * (m * sn);
    }
    Kf[d * S + l] = 2.f * acc;
}
__global__ void k_conv(const u16* u, const float* Kf, const u16* dsk, u16* out) {
    int d = blockIdx.x;
    __shared__ float su[S], sk[S];
    for (int i = threadIdx.x; i < S; i += 256) { su[i] = bf2f(u[i * D + d]); sk[i] = Kf[d * S + i]; }
    __syncthreads();
    float ds = bf2f(dsk[d]);
    for (int l = threadIdx.x; l < S; l += 256) {
        float acc = 0.f;
        for (int j = 0; j <= l; ++j) acc += sk[j] * su[l - j];
        float y = rbf(acc);
        out[l * 2 * D + D + d] = f2bf(y + rbf(su[l] * ds));
    }
}

extern "C" void kernel_launch(void* const* d_in, const int* in_sizes, int n_in,
                              void* d_out, int out_size, void* d_ws, size_t ws_size,
                              hipStream_t stream) {
    const u16* x = (const u16*)d_in[0];
    const u16* wqkv = (const u16*)d_in[1];
    const u16* wo = (const u16*)d_in[2];
    const u16* u = (const u16*)d_in[3];
    const u16* lar = (const u16*)d_in[4];
    const u16* aim = (const u16*)d_in[5];
    const u16* cre = (const u16*)d_in[6];
    const u16* cim = (const u16*)d_in[7];
    const u16* ldt = (const u16*)d_in[8];
    const u16* dsk = (const u16*)d_in[9];
    u16* out = (u16*)d_out;
    char* ws = (char*)d_ws;
    u16* qkv = (u16*)ws;
    float* kv = (float*)(ws + (8 << 20));
    u16* o = (u16*)(ws + (9 << 20));
    float* Kf = (float*)(ws + (16 << 20));
    k_qkv<<<dim3(21, S / 4), 256, 0, stream>>>(x, wqkv, qkv);
    k_kv<<<dim3(H, DH), 256, 0, stream>>>(qkv, kv);
    k_o<<<S, 448, 0, stream>>>(qkv, kv, o);
    k_out<<<dim3(7, S / 4), 256, 0, stream>>>(o, wo, out);
    k_gen<<<dim3(D, S / 256), 256, 0, stream>>>(lar, aim, cre, cim, ldt, Kf);
    k_conv<<<D, 256, 0, stream>>>(u, Kf, dsk, out);
}
```

```cpp
#include <hip/hip_runtime.h>

typedef unsigned short u16;
typedef unsigned int u32;
typedef unsigned long long u64;
typedef short bf16x8 __attribute__((ext_vector_type(8)));
typedef short s16x4 __attribute__((ext_vector_type(4)));
typedef float f32x2 __attribute__((ext_vector_type(2)));
typedef float f32x4 __attribute__((ext_vector_type(4)));
typedef float f32x16 __attribute__((ext_vector_type(16)));
typedef u32 u32x2 __attribute__((ext_vector_type(2)));
typedef u32 u32x4 __attribute__((ext_vector_type(4)));
typedef __bf16 bf2_t __attribute__((ext_vector_type(2)));
#define DI __device__ __forceinline__
#define LDSAS __attribute__((address_space(3)))
typedef __amdgpu_buffer_rsrc_t rsrc_t;

constexpr u32 WS_EPOCH = 0;
constexpr u32 WS_QFLAG = 64 << 10;
constexpr u32 WS_UT = 1 << 20;
constexpr u32 WS_YT = 5 << 20;
constexpr u32 WS_PART = 9 << 20;
constexpr u32 WS_KVG = 13 << 20;
constexpr u32 WS_PQ = 14 << 20;
constexpr u32 WS_USED = 16 << 20;
constexpr u32 OOB = 0x40000000u;
constexpr int SPIN_CAP = 1 << 14;

#define SB() __builtin_amdgcn_sched_barrier(0)
#define STDECL
#define STEND
#define STV(k, v) do {} while (0)
#define ST(k) do {} while (0)

DI u32 pk2(float lo, float hi) { f32x2 v = {lo, hi}; bf2_t b = __builtin_convertvector(v, bf2_t); return __builtin_bit_cast(u32, b); }
DI float blo(u32 v) { return __uint_as_float(v << 16); }
DI float bhi(u32 v) { return __uint_as_float(v & 0xffff0000u); }
DI float rbf(float f) { return blo(pk2(f, f)); }
DI rsrc_t mk_rsrc(const void* p, u32 bytes) { return __builtin_amdgcn_make_buffer_rsrc((void*)p, 0, bytes, 0x00020000); }
DI void cbar() { asm volatile("" ::: "memory"); }
DI void bar() { asm volatile("s_waitcnt lgkmcnt(0)\n\ts_barrier" ::: "memory"); }
DI u32 mk_tag(u32 e) { return e * 0x9E3779B1u + 0x7F4A7C15u; }
typedef LDSAS char* lptr;
DI lptr lp(u32 a) { lptr p = (lptr)a; asm("" : "+v"(p)); return p; }
template <typename T> DI T ld(lptr p, int off) { return *(const LDSAS T*)(p + off); }
template <typename T> DI void st(lptr p, int off, T v) { *(LDSAS T*)(p + off) = v; }
DI u32x2 ldtr(lptr p, int off) { s16x4 r = __builtin_amdgcn_ds_read_tr16_b64_v4i16((LDSAS s16x4*)(p + off)); return __builtin_bit_cast(u32x2, r); }
DI u32 sload(const void* p) { u32 v = *(const __attribute__((address_space(4))) u32*)p; asm volatile("" : "+s"(v)); return v; }
DI u32 opq(u32 v) { asm("" : "+v"(v)); return v; }
DI void st128(rsrc_t rs, u32 voff, u32x4 v, int aux_sc1) {
    if (aux_sc1) __builtin_amdgcn_raw_buffer_store_b128(v, rs, voff, 0, 16); else __builtin_amdgcn_raw_buffer_store_b128(v, rs, voff, 0, 0);
}
#define MFMA16(a, b, c) __builtin_amdgcn_mfma_f32_16x16x32_bf16(__builtin_bit_cast(bf16x8, a), __builtin_bit_cast(bf16x8, b), (c), 0, 0, 0)
#define MFMA32(a, b, c) __builtin_amdgcn_mfma_f32_32x32x16_bf16(__builtin_bit_cast(bf16x8, a), __builtin_bit_cast(bf16x8, b), (c), 0, 0, 0)
#define PLO(b, a) __builtin_amdgcn_perm(b, a, 0x05040100u)
#define PHI_(b, a) __builtin_amdgcn_perm(b, a, 0x07060302u)

DI void gl4(rsrc_t rs, u32 voff, u32 soff, u32x4& a, u32x4& b, u32x4& c, u32x4& d) {
    asm volatile("buffer_load_dwordx4 %0, %4, %5, %6 offen sc1\n\tbuffer_load_dwordx4 %1, %4, %5, %6 offen offset:1024 sc1\n\t"
                 "buffer_load_dwordx4 %2, %4, %5, %6 offen offset:2048 sc1\n\tbuffer_load_dwordx4 %3, %4, %5, %6 offen offset:3072 sc1"
                 : "+v"(a), "+v"(b), "+v"(c), "+v"(d) : "v"(voff), "s"(rs), "s"(soff) : "memory");
}
DI void gl4_if(u32 cond, rsrc_t rs, u32 voff, u32 soff, u32x4& a, u32x4& b, u32x4& c, u32x4& d) {
    asm volatile("s_cmp_eq_u32 %7, 0\n\ts_cbranch_scc1 1f\n\t"
                 "buffer_load_dwordx4 %0, %4, %5, %6 offen sc1\n\tbuffer_load_dwordx4 %1, %4, %5, %6 offen offset:1024 sc1\n\t"
                 "buffer_load_dwordx4 %2, %4, %5, %6 offen offset:2048 sc1\n\tbuffer_load_dwordx4 %3, %4, %5, %6 offen offset:3072 sc1\n1:"
                 : "+v"(a), "+v"(b), "+v"(c), "+v"(d) : "v"(voff), "s"(rs), "s"(soff), "s"(cond) : "memory", "scc");
}
DI void vm0(u32x4& a, u32x4& b, u32x4& c, u32x4& d) { asm volatile("s_waitcnt vmcnt(0)" : "+v"(a), "+v"(b), "+v"(c), "+v"(d) :: "memory"); }
DI void tie(u32x4& a, u32x4& b, u32x4& c, u32x4& d) { asm volatile("" : "+v"(a), "+v"(b), "+v"(c), "+v"(d) :: "memory"); }
template <int N>
DI void gload(rsrc_t rs, u32 voff, u32 soff, u32 gstride, u32x4 (&v)[N]) {
#pragma unroll
    for (int k = 0; k < N; k += 4) gl4(rs, voff, soff + (k / 4) * gstride, v[k], v[k + 1], v[k + 2], v[k + 3]);
}
template <int N>
DI int gwait(rsrc_t rs, u32 voff, u32 soff, u32 gstride, u32 tag, u32x4 (&v)[N]) {
    int spin = 0;
#pragma unroll 1
    for (; spin < SPIN_CAP; ++spin) {
        vm0(v[0], v[1], v[2], v[3]);
#pragma unroll
        for (int k = 4; k < N; k += 4) tie(v[k], v[k + 1], v[k + 2], v[k + 3]);
        u32 bad = 0;
#pragma unroll
        for (int k = 0; k < N; ++k) bad |= (v[k].y ^ tag) | (v[k].w ^ tag);
        if (__builtin_amdgcn_ballot_w64(bad != 0) == 0) break;
        gload<N>(rs, voff, soff, gstride, v);
    }
    vm0(v[0], v[1], v[2], v[3]);
#pragma unroll
    for (int k = 4; k < N; k += 4) tie(v[k], v[k + 1], v[k + 2], v[k + 3]);
    return spin;
}

struct Cm { f32x2 w, r; };
DI Cm mult(f32x2 w) { return {w, (f32x2){-w.y, w.x}}; }
DI f32x2 cmul(f32x2 a, Cm m) { return __builtin_elementwise_fma(a.yy, m.r, a.xx * m.w); }
DI f32x2 cexp(float e2, float rev, float p) {
    float m = __builtin_amdgcn_exp2f(e2 * p);
    float f = __builtin_amdgcn_fractf(rev * p);
    return (f32x2){m * __builtin_amdgcn_cosf(f), m * __builtin_amdgcn_sinf(f)};
}
DI u32 pkc(f32x2 z) { return pk2(z.x, z.y); }

constexpr u32 B_TROW = 272, B_TW = 59 * 272, B_U = 4 * B_TW, B_UA = 4160, B_K = 4096, B_XA = 10240;
template <int R0, int R1, int R2, int R3>
DI void put4(u32 a, u32 b, u32 c, u32 d) {
    asm volatile("ds_write_addtid_b32 %0 offset:%4\n\tds_write_addtid_b32 %1 offset:%5\n\tds_write_addtid_b32 %2 offset:%6\n\tds_write_addtid_b32 %3 offset:%7"
                 :: "v"(a), "v"(b), "v"(c), "v"(d), "n"(R0 * B_TROW), "n"(R1 * B_TROW), "n"(R2 * B_TROW), "n"(R3 * B_TROW) : "memory");
}

DI void role_b(int bb, const u16* u, const u16* lar, const u16* aim, const u16* cre, const u16* cim,
               const u16* ldt, const u16* dsk, u16* out, char* ws) {
    const int tid = threadIdx.x, lane = tid & 63;
    const int w = __builtin_amdgcn_readfirstlane(tid >> 6);
    const int cg = bb % 7, bi = bb / 7;
    const int ch = w >> 1, hw = w & 1;
    const int c = cg * 64 + bi * 2 + ch;
    const int l15 = lane & 15, g = lane >> 4;
    rsrc_t rws = mk_rsrc(ws, WS_USED);
    STDECL ST(0);
    u32 po = lane * 4, ps = c * 512 + hw * 256;
    u32 p_lar = __builtin_amdgcn_raw_buffer_load_b32(mk_rsrc(lar, 448 * 512), po, ps, 0);
    u32 p_aim = __builtin_amdgcn_raw_buffer_load_b32(mk_rsrc(aim, 448 * 512), po, ps, 0);
    rsrc_t ru = mk_rsrc(u, 2048 * 448 * 2);
    u32 uo = (lane >> 3) * 896 + (lane & 7) * 16;
    u32 us = (bi * 64 + w * 16) * 896 + cg * 128;
    u32x4 t0 = __builtin_amdgcn_raw_buffer_load_b128(ru, uo, us, 0);
    u32x4 t1 = __builtin_amdgcn_raw_buffer_load_b128(ru, uo, us + 8 * 896, 0);
    u32 p_cre = __builtin_amdgcn_raw_buffer_load_b32(mk_rsrc(cre, 448 * 512), po, ps, 0);
    u32 p_cim = __builtin_amdgcn_raw_buffer_load_b32(mk_rsrc(cim, 448 * 512), po, ps, 0);
    const u32 ldt2 = sload(ldt + (c & ~1)), dsk2 = sload(dsk + (c & ~1));
    const u32 tag = mk_tag(sload(ws + WS_EPOCH + (1 + cg) * 256));
    SB();
    const u32 tb = w * B_TW;
    asm volatile("s_mov_b32 m0, %0" :: "s"(tb) : "memory");
    const float dt = rbf(expf(ch ? bhi(ldt2) : blo(ldt2)));
    const float dskip = ch ? bhi(dsk2) : blo(dsk2);
    const int sl = lane & 31;
    const u32 hrow = sl - (u32)(((sl < 16 ? 0x3376111111100000ull : 0x5555555554444333ull) >> (4 * (sl & 15))) & 7);
    const lptr trl = lp(tb + sl * B_TROW + (lane >> 5) * 16);
    const lptr trh = lp(tb + (32 + hrow) * B_TROW + (lane >> 5) * 16);
    if (tid < 8) st<u32x4>(lp(B_U + (tid >> 1) * B_UA + (tid & 1) * 16), 0, (u32x4){0u, 0u, 0u, 0u});
    {
        lptr p = lp(tb + (lane >> 3) * 160 + (lane & 7) * 16);
        lptr ra = lp(tb + (4 * g + (l15 >> 2)) * 160 + (l15 & 3) * 8);
        u32 so = WS_UT + (u32)(cg * 32 + (l15 >> 1)) * 16384 + bi * 512 + w * 128 + (l15 & 1) * 64 + g * 16;
        asm volatile("" : "+v"(p_lar), "+v"(p_aim), "+v"(p_cre), "+v"(p_cim));
        st<u32x4>(p, 0, t0);
        st<u32x4>(p, 8 * 160, t1);
        u32x2 d[4];
#pragma unroll
        for (int k = 0; k < 4; ++k) d[k] = ldtr(ra, k * 32);
        SB();
#pragma unroll
        for (int k = 0; k < 4; ++k) st128(rws, so + k * 8 * 16384, (u32x4){d[k].x, tag, d[k].y, tag}, 1);
    }
    ST(1);
    f32x16 kd;
#pragma unroll
    for (int i = 0; i < 16; ++i) kd[i] = 0.f;
    u32x4 gv[4];
#pragma unroll
    for (int k = 0; k < 4; ++k) asm volatile("" : "=v"(gv[k]));
    const u32 gs = WS_UT + (cg * 32 + bi) * 16384 + w * 4096;
#pragma unroll 1
    for (int rnd = 0; rnd < 2; ++rnd) {
        float e2 = -rbf(expf(blo(p_lar))) * dt * 1.44269504088896f;
        float rev = blo(p_aim) * dt * 0.159154943091895f;
        f32x2 C = {2.f * dt * blo(p_cre), -2.f * dt * blo(p_cim)};
        p_lar >>= 16; p_aim >>= 16; p_cre >>= 16; p_cim >>= 16;
        Cm w1 = mult(cexp(e2, -rev, 1.f)), w8 = mult(cexp(e2, rev, 8.f)), w256 = mult(cexp(e2, -rev, 256.f));
        Cm w2 = mult(cmul(w1.w, w1)), w4 = mult(cmul(w2.w, w2));
        Cm w8c = mult((f32x2){w8.w.x, -w8.w.y});
        f32x2 L = w8.w, H = C, G = C;
        u32 a, b, cc, d;
#define LN (L = cmul(L, w8), pkc(L))
#define HN(m) (H = cmul(H, m), pkc(H))
#define GN (G = cmul(G, w256), H = G, pkc(G))
        a = 0x00003f80u; b = pkc(L); cc = pkc(H); d = HN(w1);  put4<0, 1, 32, 33>(a, b, cc, d);
        a = LN; b = HN(w1); cc = LN; d = HN(w1);               put4<2, 34, 3, 35>(a, b, cc, d);
        a = LN; b = HN(w1); cc = LN; d = HN(w1);               put4<4, 39, 5, 40>(a, b, cc, d);
        a = LN; b = HN(w1); cc = LN; d = HN(w1);               put4<6, 41, 7, 42>(a, b, cc, d);
        a = LN; b = GN; cc = LN; d = HN(w2);                   put4<8, 36, 9, 37>(a, b, cc, d);
        a = LN; b = HN(w2); cc = LN; d = HN(w2);               put4<10, 38, 11, 43>(a, b, cc, d);
        a = LN; b = HN(w2); cc = LN; d = GN;                   put4<12, 44, 13, 45>(a, b, cc, d);
        a = LN; b = HN(w4); cc = LN; d = HN(w4);               put4<14, 46, 15, 47>(a, b, cc, d);
        a = LN; b = GN; cc = LN; d = HN(w4);                   put4<16, 48, 17, 49>(a, b, cc, d);
        a = LN; b = HN(w4); cc = LN; d = GN;                   put4<18, 50, 19, 51>(a, b, cc, d);
        a = LN; b = HN(w8c); cc = LN; d = GN;                  put4<20, 52, 21, 53>(a, b, cc, d);
        a = LN; b = HN(w8c); cc = LN; d = GN;                  put4<22, 54, 23, 55>(a, b, cc, d);
        a = LN; b = HN(w8c); cc = LN; d = GN;                  put4<24, 56, 25, 57>(a, b, cc, d);
        a = LN; b = HN(w8c); cc = LN; d = LN;                  put4<26, 58, 27, 28>(a, b, cc, d);
        a = LN; b = LN; cc = LN;                               put4<29, 30, 31, 31>(a, b, cc, cc);
#undef LN
#undef HN
#undef GN
        if (rnd) ST(11); else ST(10);
        u32x4 lo[8], hi[8];
#pragma unroll
        for (int s = 0; s < 8; ++s) { hi[s] = ld<u32x4>(trh, s * 32); lo[s] = ld<u32x4>(trl, s * 32); }
        SB();
        gl4_if(rnd, rws, lane * 16, gs, gv[0], gv[1], gv[2], gv[3]);
#pragma unroll
        for (int s = 0; s < 8; ++s) kd = MFMA32(hi[s], lo[s], kd);
        cbar();
    }
    ST(2);
    {
        const lptr xw = lp(tb + lane * 16);
        f32x4 s0, s1;
#pragma unroll
        for (int i = 0; i < 4; ++i) { s0[i] = hw ? kd[i] : kd[8 + i]; s1[i] = hw ? kd[4 + i] : kd[12 + i]; }
        st<f32x4>(xw, 0, s0);
        st<f32x4>(xw, 1024, s1);
    }
    {
        const lptr ua = lp(B_U + ((lane >> 2) & 1) * 2 * B_UA + 30 + w * 1024 + (lane >> 3) * 32 + (lane & 3) * 8);
        int sp = gwait<4>(rws, lane * 16, gs, 0, tag, gv);
        STV(12, sp);
#pragma unroll
        for (int k = 0; k < 4; ++k) {
            st<u32x2>(ua, 2 + k * 256, (u32x2){gv[k].x, gv[k].z});
            st<u16>(ua, B_UA + k * 256, (u16)gv[k].x);
            st<u32>(ua, B_UA + 2 + k * 256, __builtin_amdgcn_alignbit(gv[k].z, gv[k].x, 16));
            st<u16>(ua, B_UA + 6 + k * 256, (u16)(gv[k].z >> 16));
        }
    }
    bar();
    ST(3);
    u32x4 ua[2][8], kf[2][8];
    lptr up[4];
    {
        const u32 ab = B_U + (ch * 2 + (~l15 & 1)) * B_UA + 32 + (64 * hw + 4 * g + ((l15 - 15) >> 1)) * 4;
#pragma unroll
        for (int i2 = 0; i2 < 4; ++i2) up[i2] = lp(ab + i2 * 1024);
    }
#define LDUA(buf, rr) _Pragma("unroll") for (int i = 0; i < 8; ++i) _Pragma("unroll") for (int q = 0; q < 4; ++q) \
        ua[buf][i][q] = ld<u32>(up[i >> 1], ((i & 1) * 128 + (rr) * 16 + q) * 4);
    {
        const lptr xr = lp((w ^ 1) * B_TW + lane * 16);
        f32x4 p0 = ld<f32x4>(xr, 0), p1 = ld<f32x4>(xr, 1024);
        LDUA(0, 0)
        SB();
        const int j = lane & 31, h = lane >> 5;
        const u32 kb = ch * 2 * B_TW + B_K;
        if (hw == 0) {
#pragma unroll
            for (int i = 0; i < 4; ++i) { p0[i] += kd[i]; p1[i] += kd[4 + i]; }
            st<u32x4>(lp(kb + 4080 - 16 * lane), 0, (u32x4){pk2(p1[3], p1[2]), pk2(p1[1], p1[0]), pk2(p0[3], p0[2]), pk2(p0[1], p0[0])});
            if (lane < 30) st<u32x4>(lp(kb + 4096 + 16 * lane), 0, (u32x4){0u, 0u, 0u, 0u});
        } else {
#pragma unroll
            for (int i = 0; i < 4; ++i) { p0[i] += kd[8 + i]; p1[i] += kd[12 + i]; }
            st<u32x4>(lp(kb + 4080 - 1024 - 16 * lane), 0, (u32x4){pk2(p0[2], p0[2]), pk2(p0[1], p0[1]), pk2(p0[1], p0[0]), pk2(p0[0], p0[0])});
            const lptr k3 = lp(kb + 4080 - 2048 - 512 - 16 * j - 1024 * h);
            u32 aa = pk2(p1[0], p1[0]), bb2 = pk2(p1[2], p1[2]);
            st<u32x4>(k3, 512, (u32x4){pk2(p1[1], p1[1]), pk2(p1[1], (j & 1) ? p1[1] : p1[0]), aa, aa});
            st<u32x4>(k3, 0, (u32x4){pk2(p1[3], p1[3]), pk2(p1[3], (j & 1) ? p1[3] : p1[2]), bb2, bb2});
        }
    }
    bar();
    ST(4);
    f32x4 acc[8];
    f32x4 sk[4];
#pragma unroll
    for (int i = 0; i < 8; ++i) acc[i] = (f32x4){0.f, 0.f, 0.f, 0.f};
    {
        const lptr kp = lp(ch * 2 * B_TW + B_K + (2032 - 1792 + 128 * hw - 16 * l15 + 8 * g) * 2);
        const lptr sp = lp(B_U + ch * 2 * B_UA + 32 + hw * 2048 + 32 * l15 + 8 * g);
        u32x2 su[4];
#pragma unroll
        for (int q = 0; q < 8; ++q) kf[0][q] = ld<u32x4>(kp, (7 - q) * 512);
#pragma unroll
        for (int rr = 0; rr < 4; ++rr) {
            if (rr < 3) {
                LDUA((rr + 1) & 1, rr + 1)
#pragma unroll
                for (int q = 0; q < 8; ++q) kf[(rr + 1) & 1][q] = ld<u32x4>(kp, (rr + 1) * 64 + (7 - q) * 512);
            } else {
#pragma unroll
                for (int k = 0; k < 4; ++k) su[k] = ld<u32x2>(sp, k * 512);
            }
#pragma unroll
            for (int q = 0; q < 8; ++q)
#pragma unroll
                for (int i = 0; i + q < 8; ++i) acc[i + q] = MFMA16(ua[rr & 1][i], kf[rr & 1][q], acc[i + q]);
            if (rr < 3) {
#pragma unroll
                for (int n = 0; n < 12; ++n) { __builtin_amdgcn_sched_group_barrier(0x008, 2, 0); __builtin_amdgcn_sched_group_barrier(0x100, 2, 0); }
            } else {
#pragma unroll
                for (int k = 0; k < 4; ++k) {
                    u32 a = pk2(blo(su[k].x) * dskip, bhi(su[k].x) * dskip), b = pk2(blo(su[k].y) * dskip, bhi(su[k].y) * dskip);
                    sk[k] = (f32x4){blo(a), bhi(a), blo(b), bhi(b)};
                }
            }
            SB();
        }
    }
#undef LDUA
    ST(5);
    {
        const lptr xw = lp(tb + B_XA + lane * 16), xr = lp((w ^ 1) * B_TW + B_XA + lane * 16);
        if (hw) {
#pragma unroll
            for (int k = 0; k < 4; ++k) { st<f32x4>(xw, k * 1024, acc[k]); acc[k] = acc[4 + k]; }
            cbar();
        } else {
#pragma unroll
            for (int k = 0; k < 4; ++k) st<f32x4>(xw, k * 1024, acc[4 + k]);
        }
        const u32 so = WS_YT + (u32)(cg * 32 + hw * 16 + (l15 >> 2)) * 16384 + bi * 512 + ch * 256 + ((l15 & 3) * 4 + g) * 16;
        bar();
        f32x4 o[4];
#pragma unroll
        for (int k = 0; k < 4; ++k) o[k] = ld<f32x4>(xr, k * 1024);
        SB();
#pragma unroll
        for (int k = 0; k < 4; ++k) {
            f32x4 y = acc[k] + o[k];
            u32 y01 = pk2(y[0], y[1]), y23 = pk2(y[2], y[3]);
            u32 o01 = pk2(blo(y01) + sk[k][0], bhi(y01) + sk[k][1]), o23 = pk2(blo(y23) + sk[k][2], bhi(y23) + sk[k][3]);
            st128(rws, so + k * 65536, (u32x4){o01, tag, o23, tag}, 1);
        }
    }
    ST(6);
    {
        const u32 ys = WS_YT + (cg * 32 + bi) * 16384 + w * 4096;
        gload<4>(rws, lane * 16, ys, 0, gv);
        const lptr sa = lp(B_U + (w * 16 + g) * 160 + l15 * 8);
        int sp = gwait<4>(rws, lane * 16, ys, 0, tag, gv);
        STV(13, sp);
#pragma unroll
        for (int k = 0; k < 4; ++k) st<u32x2>(sa, k * 4 * 160, (u32x2){gv[k].x, gv[k].z});
        bar();
        ST(7);
        rsrc_t ro = mk_rsrc(out, 2048 * 896 * 2);
        const lptr ra = lp(B_U + (8 * g + (l15 >> 2)) * 160 + w * 32 + (l15 & 3) * 8);
        const u32 oo = (u32)(bi * 64 + w * 16 + l15) * 1792 + 896 + cg * 128 + g * 16;
        u32x2 d[4];
#pragma unroll
        for (int k = 0; k < 4; ++k) d[k] = ldtr(ra, (k >> 1) * 32 * 160 + (k & 1) * 4 * 160);
        SB();
#pragma unroll
        for (int k = 0; k < 2; ++k) st128(ro, oo + k * 64, (u32x4){d[2 * k].x, d[2 * k].y, d[2 * k + 1].x, d[2 * k + 1].y}, 0);
        if (bi == 0 && tid == 0) __builtin_amdgcn_raw_buffer_store_b32(tag, rws, 0, WS_EPOCH + (1 + cg) * 256, 16);
    }
    ST(8);
    STEND;
}

constexpr u32 A_KV = 57344, A_KVM = 12288, A_KVF = 49152;
DI void dma16(rsrc_t rs, u32 voff, u32 soff, u32 lbase, int loff) {
    asm volatile("s_add_u32 m0, %3, %4\n\ts_nop 0\n\tbuffer_load_dwordx4 %0, %1, %2 offen lds"
                 :: "v"(voff), "s"(rs), "s"(soff), "s"(lbase), "n"(loff) : "memory", "scc");
}
DI float phi(float t) {
    float e = __builtin_amdgcn_exp2f(__builtin_fmaf(t, 1.44269504088896f, -1.44269504088896e-6f));
    return __builtin_amdgcn_fmed3f(e, 0.f, 1.f) + __builtin_fmaxf(t, 1e-6f);
}

DI void role_a(int b, const u16* x, const u16* wqkv, const u16* wo, u16* out, char* ws) {
    const int tid = threadIdx.x, lane = tid & 63;
    const int w = __builtin_amdgcn_readfirstlane(tid >> 6);
    const int l15 = lane & 15, g = lane >> 4;
    const int j = (b >> 3) & 7, rgl = b >> 6, rg = (b & 7) * 4 + rgl;
    const int line = j < 6 ? (w < 2 ? 7 : 12) + j + w : (j - 6) * 4 + w;
    const bool gemm = !(j == 7 && w == 3);
    rsrc_t rws = mk_rsrc(ws, WS_USED);
    STDECL ST(0);
    u32 col[7];
#pragma unroll
    for (int cc = 0; cc < 7; ++cc) { u32 c = cc + 2 * rgl; col[cc] = c >= 7 ? c - 7 : c; }
    if (w >= 2) {
        rsrc_t rx = mk_rsrc(x, 2048 * 448 * 2);
        u32 xo[4];
#pragma unroll
        for (int rb = 0; rb < 4; ++rb) xo[rb] = (rb * 8 + (lane >> 3)) * 896 + (((lane & 7) ^ ((rb * 4 + g) & 7)) * 16);
        const u32 xs = (rg * 64 + (w - 2) * 32) * 896, lb = (w - 2) * 4096;
#pragma unroll
        for (int cc = 0; cc < 7; ++cc)
#pragma unroll
            for (int rb = 0; rb < 4; ++rb) dma16(rx, xo[rb], xs + col[cc] * 128, lb, cc * 8192 + rb * 1024);
    }
    const u32 tag = mk_tag(sload(ws + WS_EPOCH));
    const u32 qtag = mk_tag(sload(ws + WS_EPOCH + (8 + rg) * 256));
    f32x4 acc[4][4];
    if (gemm) {
        rsrc_t rw = mk_rsrc(wqkv, 448 * 2688);
        u32 wvo[4];
#pragma unroll
        for (int p = 0; p < 4; ++p) wvo[p] = opq((8 * g + 2 * p) * 2688 + l15 * 8);
        u32x2 wv[14][8];
#define LDW(s) _Pragma("unroll") for (int r = 0; r < 8; ++r) \
            wv[s][r] = __builtin_amdgcn_raw_buffer_load_b64(rw, wvo[r >> 1] + (r & 1) * 2688, line * 128 + (col[(s) >> 1] * 64 + ((s) & 1) * 32) * 2688, 0);
#pragma unroll
        for (int s = 0; s < 7; ++s) { LDW(s) }
        const float ini = (j < 6 && w >= 2) ? 0.f : 1e-6f;
#pragma unroll
        for (int rt = 0; rt < 4; ++rt)
#pragma unroll
            for (int jj = 0; jj < 4; ++jj) acc[rt][jj] = (f32x4){ini, ini, ini, ini};
        const lptr xp0 = lp(l15 * 128 + ((g ^ (l15 >> 1)) * 16)), xp1 = lp(l15 * 128 + (((4 + g) ^ (l15 >> 1)) * 16));
        SB();
        asm volatile("s_waitcnt vmcnt(56) lgkmcnt(0)\n\ts_barrier" ::: "memory");
        ST(1);
        u32x4 xf[2][4];
#pragma unroll
        for (int rt = 0; rt < 4; ++rt) xf[0][rt] = ld<u32x4>(xp0, rt * 2048);
#pragma unroll
        for (int s = 0; s < 14; ++s) {
            if (s < 13) {
#pragma unroll
                for (int rt = 0; rt < 4; ++rt) xf[(s + 1) & 1][rt] = ld<u32x4>((s & 1) ? xp0 : xp1, ((s + 1) >> 1) * 8192 + rt * 2048);
            }
            u32x4 wf[4];
#pragma unroll
            for (int p = 0; p < 4; ++p) {
                wf[0][p] = PLO(wv[s][2 * p + 1].x, wv[s][2 * p].x); wf[1][p] = PHI_(wv[s][2 * p + 1].x, wv[s][2 * p].x);
                wf[2][p] = PLO(wv[s][2 * p + 1].y, wv[s][2 * p].y); wf[3][p] = PHI_(wv[s][2 * p + 1].y, wv[s][2 * p].y);
            }
            if (s < 7) { LDW(s + 7) }
#pragma unroll
            for (int jj = 0; jj < 4; ++jj)
#pragma unroll
                for (int rt = 0; rt < 4; ++rt) acc[rt][jj] = MFMA16(xf[s & 1][rt], wf[jj], acc[rt][jj]);
            SB();
        }
#undef LDW
    } else {
        asm volatile("s_waitcnt vmcnt(0) lgkmcnt(0)\n\ts_barrier" ::: "memory");
    }
    ST(2);
    const int cga = j < 6 ? j : 6;
    u32x2 wov[28];
    if (j != 6) {
        rsrc_t ro = mk_rsrc(wo, 448 * 896);
        const u32 vo = opq(7 * g * 896 + l15 * 8);
#pragma unroll
        for (int n = 0; n < 28; ++n)
            wov[n] = __builtin_amdgcn_raw_buffer_load_b64(ro, vo + (n % 7 % 4) * 896, (w * 112 + (n / 7) * 28 + (n % 7 / 4) * 4) * 896 + cga * 128, 0);
    }
    if (j < 6) {
        const int h0 = j < 4 ? 3 * j : 2 * j + 3, nh = (j == 3 || j == 4) ? 2 : 3;
        const int ir = (w & 1) * 16 + l15 - (7 * h0 - 16 * j);
        if (w < 2) {
#pragma unroll
            for (int rt = 0; rt < 4; ++rt)
#pragma unroll
                for (int jj = 0; jj < 4; ++jj)
#pragma unroll
                    for (int i = 0; i < 4; ++i) acc[rt][jj][i] = phi(acc[rt][jj][i]);
        }
        if ((u32)ir < 21u) {
            const lptr p = lp(A_KV + (w >> 1) * A_KVM + g * 1536 + (4 * ir + (ir >> 1)) * 16);
#pragma unroll
            for (int kc = 0; kc < 2; ++kc)
#pragma unroll
                for (int jj = 0; jj < 4; ++jj)
                    st<u32x4>(p, kc * 6144 + jj * 16, (u32x4){pk2(acc[2 * kc][jj][0], acc[2 * kc][jj][1]), pk2(acc[2 * kc][jj][2], acc[2 * kc][jj][3]),
                                                           pk2(acc[2 * kc + 1][jj][0], acc[2 * kc + 1][jj][1]), pk2(acc[2 * kc + 1][jj][2], acc[2 * kc + 1][jj][3])});
        }
        const int n = lane & 31, h2 = lane >> 5;
        const int ck = 28 * w + n, slot = (n >> 4) * 4 + (n & 3), cv = 28 * w + 7 * ((n >> 2) & 3) + slot;
        const lptr kp = lp(A_KV + h2 * 1536 + (ck + (ck >> 3)) * 16), vp = lp(A_KV + A_KVM + h2 * 1536 + (cv + (cv >> 3)) * 16);
        const u32 so = WS_PART + (u32)((h0 + w) * 14 + h2 * 2 + (n >> 4)) * 16384 + rg * 512 + l15 * 16;
        ST(3);
        bar();
        ST(4);
        if (w < nh) {
            u32x4 kf[4], vf[4];
#pragma unroll
            for (int s = 0; s < 4; ++s) { kf[s] = ld<u32x4>(kp, s * 3072); vf[s] = ld<u32x4>(vp, s * 3072); }
            f32x16 d;
#pragma unroll
            for (int i = 0; i < 16; ++i) d[i] = 0.f;
#pragma unroll
            for (int s = 0; s < 4; ++s) {
#pragma unroll
                for (int q = 0; q < 4; ++q) vf[s][q] = slot == 7 ? 0x3f803f80u : vf[s][q];
                d = MFMA32(kf[s], vf[s], d);
            }
#pragma unroll
            for (int q = 0; q < 4; ++q)
#pragma unroll
                for (int dp = 0; dp < 2; ++dp)
                    st128(rws, so + q * 65536 + dp * 256 + (q == 3 ? h2 * OOB : 0),
                          (u32x4){__float_as_uint(d[4 * q + 2 * dp]), tag, __float_as_uint(d[4 * q + 2 * dp + 1]), tag}, 1);
        }
    } else {
        const int Q = line * 16 + l15, hd = (Q * 147) >> 10, sl = Q - 7 * hd + 1;
        const int hb = j == 6 ? 0 : 9;
        const lptr p = lp(((hd - hb) * 4 + (sl >> 1)) * 272 + (sl & 1) * 8 + g * 64);
        u32x2 pv[4][4];
        if (gemm) {
#pragma unroll
            for (int rt = 0; rt < 4; ++rt)
#pragma unroll
                for (int i = 0; i < 4; ++i)
                    pv[rt][i] = (u32x2){pk2(phi(acc[rt][0][i]), phi(acc[rt][1][i])), pk2(phi(acc[rt][2][i]), phi(acc[rt][3][i]))};
        }
        ST(3);
        bar();
        if (gemm) {
#pragma unroll
            for (int rt = 0; rt < 4; ++rt)
#pragma unroll
                for (int i = 0; i < 4; ++i) st<u32x2>(p, rt * 10880 + i * 16, pv[rt][i]);
        }
        bar();
        ST(4);
        const lptr rp = lp(w * 10880 + g * 272 + l15 * 16);
        const u32 msk = g ? ~0u : 0u;
        const u32 so = WS_PQ + rg * 65536 + w * 16384 + hb * 1024 + lane * 16;
        u32x4 f[10];
#pragma unroll
        for (int h = 0; h < 10; ++h) f[h] = ld<u32x4>(rp, h * 1088);
#pragma unroll
        for (int h = 0; h < 10; ++h) {
            f[h].x &= msk; f[h].y &= msk;
            u32 skip = h == 0 ? ((j == 7 && g == 0) ? OOB : 0) : (h == 9 ? (g ? OOB : 0) : 0);
            if (h < 7 || j == 6) st128(rws, so + h * 1024 + skip, f[h], 1);
        }
        asm volatile("s_waitcnt vmcnt(0)\n\ts_barrier" ::: "memory");
        if (tid == 0) __builtin_amdgcn_raw_buffer_store_b32(qtag, rws, 0, WS_QFLAG + (rg * 2 + j - 6) * 256, 16);
        if (j == 6) { ST(5); STEND; return; }
    }
    ST(5);
    u32x4 gv[16];
#pragma unroll
    for (int k = 0; k < 16; ++k) asm volatile("" : "=v"(gv[k]));
    if (w == 3) {
        const int rho = rg * 7 + cga;
        const u32 ps = WS_PART + rho * 16384;
        gload<16>(rws, lane * 16, ps, 4096, gv);
        int sp = gwait<16>(rws, lane * 16, ps, 4096, tag, gv);
        STV(12, sp);
        float s0 = 0.f, s1 = 0.f;
#pragma unroll
        for (int k = 0; k < 16; ++k) { s0 += __uint_as_float(gv[k].x); s1 += __uint_as_float(gv[k].z); }
        auto r0 = __builtin_amdgcn_permlane32_swap(__float_as_uint(s0), __float_as_uint(s0), false, false);
        auto r1 = __builtin_amdgcn_permlane32_swap(__float_as_uint(s1), __float_as_uint(s1), false, false);
        u32 pr = pk2(__uint_as_float(r0[0]) + __uint_as_float(r0[1]), __uint_as_float(r1[0]) + __uint_as_float(r1[1]));
        const int hq = rho >> 1, hh = (hq * 147) >> 10;
        __builtin_amdgcn_raw_buffer_store_b64((u32x2){pr, tag}, rws, l15 * 16 + (g & 1) * 8 + (g >> 1) * OOB,
                                              WS_KVG + (hq + hh + 1) * 512 + (rho & 1) * 256, 16);
        ST(6);
    }
    {
        const u32 hf = 7 * w;
        const lptr pe = lp((hf >> 1) * 4096 + (hf & 1) * 8 + lane * 16), po = lp(((hf + 1) >> 1) * 4096 + ((hf + 1) & 1) * 8 + lane * 16);
#pragma unroll
        for (int n = 0; n < 7; ++n) {
            const lptr p = (n & 1) ? po : pe;
            const int o = (n >> 1) * 4096;
            const u32x2 a = wov[4 * n], bq = wov[4 * n + 1], c = wov[4 * n + 2], d = wov[4 * n + 3];
            st<u32x2>(p, o, (u32x2){PLO(bq.x, a.x), PLO(d.x, c.x)});
            st<u32x2>(p, o + 1024, (u32x2){PHI_(bq.x, a.x), PHI_(d.x, c.x)});
            st<u32x2>(p, o + 2048, (u32x2){PLO(bq.y, a.y), PLO(d.y, c.y)});
            st<u32x2>(p, o + 3072, (u32x2){PHI_(bq.y, a.y), PHI_(d.y, c.y)});
        }
    }
    bar();
    const lptr fp = lp(lane * 16);
    u32x4 wr[6][4];
#pragma unroll
    for (int s = 0; s < 6; ++s)
#pragma unroll
        for (int jj = 0; jj < 4; ++jj) wr[s][jj] = ld<u32x4>(fp, (8 + s) * 4096 + jj * 1024);
    bar();
    ST(7);
    u32x4 pq[16];
    {
        int spin = 0;
#pragma unroll 1
        for (; spin < SPIN_CAP; ++spin) {
            cbar();
            u32 f = __builtin_amdgcn_raw_buffer_load_b32(rws, (lane & 1) * 256, WS_QFLAG + rg * 512, 16);
            if (__builtin_amdgcn_ballot_w64(f != qtag) == 0) break;
            __builtin_amdgcn_s_sleep(2);
        }
        cbar();
        STV(13, spin);
#pragma unroll
        for (int k = 0; k < 16; ++k) asm volatile("" : "=v"(pq[k]));
        gload<16>(rws, lane * 16, WS_PQ + rg * 65536 + w * 16384, 4096, pq);
    }
    ST(8);
    {
        const u32 vo = lane * 16;
        const u32 etag = lane < 32 ? 0u : tag;
        const u32 ks = WS_KVG + w * 16384;
        int spin = 0;
#pragma unroll 1
        for (; spin < SPIN_CAP; ++spin) {
            gload<16>(rws, vo, ks, 4096, gv);
            vm0(gv[0], gv[1], gv[2], gv[3]);
#pragma unroll
            for (int k = 4; k < 16; k += 4) tie(gv[k], gv[k + 1], gv[k + 2], gv[k + 3]);
            u32 bad = 0, bad0 = 0;
#pragma unroll
            for (int k = 0; k < 16; ++k) { if (k & 3) bad |= (gv[k].y ^ tag) | (gv[k].w ^ tag); else bad0 |= (gv[k].y ^ tag) | (gv[k].w ^ tag); }
            if (__builtin_amdgcn_ballot_w64(bad != 0 || (bad0 != 0 && lane >= 32)) == 0) break;
        }
        STV(14, spin);
#pragma unroll
        for (int k = 0; k < 16; k += 4) tie(pq[k], pq[k + 1], pq[k + 2], pq[k + 3]);
        const lptr p = lp(A_KVF + w * 8192 + (g & 1) * 1024 + l15 * 16 + (g >> 1) * 8);
        const u32 msk = etag ? ~0u : (lane < 32 ? 0u : ~0u);
#pragma unroll
        for (int k = 0; k < 16; ++k) {
            u32x2 v = {gv[k].x, gv[k].z};
            if ((k & 3) == 0) { v.x &= msk; v.y &= msk; }
            st<u32x2>(p, (k >> 2) * 2048 + (k & 3) * 256, v);
        }
    }
    bar();
    ST(9);
    {
        f32x4 oc[4];
#pragma unroll
        for (int jj = 0; jj < 4; ++jj) oc[jj] = (f32x4){0.f, 0.f, 0.f, 0.f};
        float ov[112];
#pragma unroll
        for (int h = 0; h < 16; ++h) {
            u32x4 k0 = ld<u32x4>(fp, A_KVF + h * 2048), k1 = ld<u32x4>(fp, A_KVF + h * 2048 + 1024);
            f32x4 n0 = MFMA16(k0, pq[h], ((f32x4){0.f, 0.f, 0.f, 0.f}));
            f32x4 n1 = MFMA16(k1, pq[h], ((f32x4){0.f, 0.f, 0.f, 1e-6f}));
            float r = __builtin_amdgcn_rcpf(n1[3]);
#pragma unroll
            for (int i = 0; i < 4; ++i) ov[7 * h + i] = n0[i] * r;
#pragma unroll
            for (int i = 0; i < 3; ++i) ov[7 * h + 4 + i] = n1[i] * r;
#pragma unroll
            for (int s = 0; s < 14; ++s) {
                if (8 * s + 7 <= 7 * h + 6 && 8 * s + 7 > 7 * h - 1) {
                    u32x4 of = {pk2(ov[8 * s], ov[8 * s + 1]), pk2(ov[8 * s + 2], ov[8 * s + 3]), pk2(ov[8 * s + 4], ov[8 * s + 5]), pk2(ov[8 * s + 6], ov[8 * s + 7])};
#pragma unroll
                    for (int jj = 0; jj < 4; ++jj) {
                        u32x4 wf = s < 8 ? ld<u32x4>(fp, s * 4096 + jj * 1024) : wr[s < 8 ? 0 : s - 8][jj];
                        oc[jj] = MFMA16(of, wf, oc[jj]);
                    }
                }
            }
        }
        rsrc_t ro = mk_rsrc(out, 2048 * 896 * 2);
        const u32 oo = (u32)(rg * 64 + w * 16 + 4 * g) * 1792 + cga * 128 + l15 * 8;
#pragma unroll
        for (int i = 0; i < 4; ++i)
            __builtin_amdgcn_raw_buffer_store_b64((u32x2){pk2(oc[0][i], oc[1][i]), pk2(oc[2][i], oc[3][i])}, ro, oo + (i & 1) * 1792, (i >> 1) * 3584, 0);
        if (j == 0 && tid == 0) {
            __builtin_amdgcn_raw_buffer_store_b32(qtag, rws, 0, WS_EPOCH + (8 + rg) * 256, 16);
            if (b == 0) __builtin_amdgcn_raw_buffer_store_b32(tag, rws, 0, WS_EPOCH, 16);
        }
    }
    ST(10);
    STEND;
}

__global__ __launch_bounds__(256, 2) void fused_kernel(const u16* x, const u16* wqkv, const u16* wo, const u16* u,
        const u16* lar, const u16* aim, const u16* cre, const u16* cim, const u16* ldt, const u16* dsk, u16* out, char* ws) {
    __shared__ __attribute__((aligned(16))) char smem[81920];
    asm volatile("" :: "v"((u32)(size_t)(LDSAS char*)smem) : "memory");
    const int b = blockIdx.x;
    if (b >= 256) {
        if (b < 256 + 224) role_b(b - 256, u, lar, aim, cre, cim, ldt, dsk, out, ws);
    } else {
        role_a(b, x, wqkv, wo, out, ws);
    }
}

extern "C" void kernel_launch(void* const* d_in, const int* in_sizes, int n_in, void* d_out, int out_size,
                              void* d_ws, size_t ws_size, hipStream_t stream) {
    fused_kernel<<<512, 256, 0, stream>>>((const u16*)d_in[0], (const u16*)d_in[1], (const u16*)d_in[2], (const u16*)d_in[3],
        (const u16*)d_in[4], (const u16*)d_in[5], (const u16*)d_in[6], (const u16*)d_in[7], (const u16*)d_in[8],
        (const u16*)d_in[9], (u16*)d_out, (char*)d_ws);
}
```

```cpp
#include <hip/hip_runtime.h>

typedef unsigned short u16;
typedef unsigned int u32;
typedef unsigned long long u64;
typedef short bf16x8 __attribute__((ext_vector_type(8)));
typedef short s16x4 __attribute__((ext_vector_type(4)));
typedef float f32x2 __attribute__((ext_vector_type(2)));
typedef float f32x4 __attribute__((ext_vector_type(4)));
typedef float f32x16 __attribute__((ext_vector_type(16)));
typedef u32 u32x2 __attribute__((ext_vector_type(2)));
typedef u32 u32x4 __attribute__((ext_vector_type(4)));
typedef __bf16 bf2_t __attribute__((ext_vector_type(2)));
#define DI __device__ __forceinline__
#define LDSAS __attribute__((address_space(3)))
typedef __amdgpu_buffer_rsrc_t rsrc_t;

constexpr u32 WS_EPOCH = 0;
constexpr u32 WS_CFLAG = 64 << 10;
constexpr u32 WS_NOTE = 68 << 10;
constexpr u32 WS_UT = 1 << 20;
constexpr u32 WS_YT = 5 << 20;
constexpr u32 WS_PART = 9 << 20;
constexpr u32 WS_KVG = 12 << 20;
constexpr u32 WS_PQ = 10 << 20;
constexpr u32 WS_USED = 16 << 20;
constexpr u32 OOB = 0x40000000u;
constexpr u32 SENT = 0xffffffffu;
constexpr int SPIN_CAP = 1 << 14;

#define SB() __builtin_amdgcn_sched_barrier(0)
#define STDECL
#define STEND
#define STV(k, v) do {} while (0)
#define ST(k) do {} while (0)

DI u32 pk2(float lo, float hi) { f32x2 v = {lo, hi}; bf2_t b = __builtin_convertvector(v, bf2_t); return __builtin_bit_cast(u32, b); }
DI float blo(u32 v) { return __uint_as_float(v << 16); }
DI float bhi(u32 v) { return __uint_as_float(v & 0xffff0000u); }
DI float rbf(float f) { return blo(pk2(f, f)); }
DI rsrc_t mk_rsrc(const void* p, u32 bytes) { return __builtin_amdgcn_make_buffer_rsrc((void*)p, 0, bytes, 0x00020000); }
DI void cbar() { asm volatile("" ::: "memory"); }
DI void bar() { asm volatile("s_waitcnt lgkmcnt(0)\n\ts_barrier" ::: "memory"); }
DI u32 mk_tag(u32 e) { return e * 0x9E3779B1u + 0x7F4A7C15u; }
typedef LDSAS char* lptr;
DI lptr lp(u32 a) { lptr p = (lptr)a; asm("" : "+v"(p)); return p; }
template <typename T> DI T ld(lptr p, int off) { return *(const LDSAS T*)(p + off); }
template <typename T> DI void st(lptr p, int off, T v) { *(LDSAS T*)(p + off) = v; }
DI u32x2 ldtr(lptr p, int off) { s16x4 r = __builtin_amdgcn_ds_read_tr16_b64_v4i16((LDSAS s16x4*)(p + off)); return __builtin_bit_cast(u32x2, r); }
DI u32 sload(const void* p) { u32 v = *(const __attribute__((address_space(4))) u32*)p; asm volatile("" : "+s"(v)); return v; }
DI u32 sld(const void* p) { return *(const __attribute__((address_space(4))) u32*)p; }
DI u32 opq(u32 v) { asm("" : "+v"(v)); return v; }
DI void st128(rsrc_t rs, u32 voff, u32x4 v, int aux_sc1) {
    if (aux_sc1) __builtin_amdgcn_raw_buffer_store_b128(v, rs, voff, 0, 16); else __builtin_amdgcn_raw_buffer_store_b128(v, rs, voff, 0, 0);
}
#define MFMA16(a, b, c) __builtin_amdgcn_mfma_f32_16x16x32_bf16(__builtin_bit_cast(bf16x8, a), __builtin_bit_cast(bf16x8, b), (c), 0, 0, 0)
#define MFMA32(a, b, c) __builtin_amdgcn_mfma_f32_32x32x16_bf16(__builtin_bit_cast(bf16x8, a), __builtin_bit_cast(bf16x8, b), (c), 0, 0, 0)
#define PLO(b, a) __builtin_amdgcn_perm(b, a, 0x05040100u)
#define PHI_(b, a) __builtin_amdgcn_perm(b, a, 0x07060302u)

DI void gl4(rsrc_t rs, u32 voff, u32 soff, u32x4& a, u32x4& b, u32x4& c, u32x4& d) {
    asm volatile("buffer_load_dwordx4 %0, %4, %5, %6 offen sc1\n\tbuffer_load_dwordx4 %1, %4, %5, %6 offen offset:1024 sc1\n\t"
                 "buffer_load_dwordx4 %2, %4, %5, %6 offen offset:2048 sc1\n\tbuffer_load_dwordx4 %3, %4, %5, %6 offen offset:3072 sc1"
                 : "+v"(a), "+v"(b), "+v"(c), "+v"(d) : "v"(voff), "s"(rs), "s"(soff) : "memory");
}
DI void gl4_if(u32 cond, rsrc_t rs, u32 voff, u32 soff, u32x4& a, u32x4& b, u32x4& c, u32x4& d) {
    asm volatile("s_cmp_eq_u32 %7, 0\n\ts_cbranch_scc1 1f\n\t"
                 "buffer_load_dwordx4 %0, %4, %5, %6 offen sc1\n\tbuffer_load_dwordx4 %1, %4, %5, %6 offen offset:1024 sc1\n\t"
                 "buffer_load_dwordx4 %2, %4, %5, %6 offen offset:2048 sc1\n\tbuffer_load_dwordx4 %3, %4, %5, %6 offen offset:3072 sc1\n1:"
                 : "+v"(a), "+v"(b), "+v"(c), "+v"(d) : "v"(voff), "s"(rs), "s"(soff), "s"(cond) : "memory", "scc");
}
DI void vm0(u32x4& a, u32x4& b, u32x4& c, u32x4& d) { asm volatile("s_waitcnt vmcnt(0)" : "+v"(a), "+v"(b), "+v"(c), "+v"(d) :: "memory"); }
DI void tie(u32x4& a, u32x4& b, u32x4& c, u32x4& d) { asm volatile("" : "+v"(a), "+v"(b), "+v"(c), "+v"(d) :: "memory"); }
template <int N>
DI void gload(rsrc_t rs, u32 voff, u32 soff, u32 gstride, u32x4 (&v)[N]) {
#pragma unroll
    for (int k = 0; k < N; k += 4) gl4(rs, voff, soff + (k / 4) * gstride, v[k], v[k + 1], v[k + 2], v[k + 3]);
}
template <int N>
DI int gwait(rsrc_t rs, u32 voff, u32 soff, u32 gstride, u32 tag, u32x4 (&v)[N]) {
    int spin = 0;
#pragma unroll 1
    for (; spin < SPIN_CAP; ++spin) {
        vm0(v[0], v[1], v[2], v[3]);
#pragma unroll
        for (int k = 4; k < N; k += 4) tie(v[k], v[k + 1], v[k + 2], v[k + 3]);
        u32 bad = 0;
#pragma unroll
        for (int k = 0; k < N; ++k) bad |= (v[k].y ^ tag) | (v[k].w ^ tag);
        if (__builtin_amdgcn_ballot_w64(bad != 0) == 0) break;
        gload<N>(rs, voff, soff, gstride, v);
    }
    vm0(v[0], v[1], v[2], v[3]);
#pragma unroll
    for (int k = 4; k < N; k += 4) tie(v[k], v[k + 1], v[k + 2], v[k + 3]);
    return spin;
}

DI void clear_issue(rsrc_t rws, int bb, int w, int lane) {
    const u32x4 ones = {SENT, SENT, SENT, SENT};
    const u32 co = WS_PART + bb * 12288 + w * 3072 + lane * 16;
#pragma unroll
    for (int k = 0; k < 3; ++k) st128(rws, co + k * 1024, ones, 1);
    st128(rws, lane < 2 ? WS_KVG + bb * 128 + w * 32 + lane * 16 : OOB, ones, 1);
}
DI void clear_flag(rsrc_t rws, int bb, int w, int lane, u32 atag) {
    asm volatile("s_waitcnt vmcnt(0)" ::: "memory");
    __builtin_amdgcn_raw_buffer_store_b32(atag, rws, lane ? OOB : 0, WS_CFLAG + (bb * 4 + w) * 4, 16);
}

struct Cm { f32x2 w, r; };
DI Cm mult(f32x2 w) { return {w, (f32x2){-w.y, w.x}}; }
DI f32x2 cmul(f32x2 a, Cm m) { return __builtin_elementwise_fma(a.yy, m.r, a.xx * m.w); }
DI f32x2 cexp(float e2, float rev, float p) {
    float m = __builtin_amdgcn_exp2f(e2 * p);
    float f = __builtin_amdgcn_fractf(rev * p);
    return (f32x2){m * __builtin_amdgcn_cosf(f), m * __builtin_amdgcn_sinf(f)};
}
DI u32 pkc(f32x2 z) { return pk2(z.x, z.y); }

constexpr u32 B_TROW = 272, B_TW = 59 * 272, B_U = 4 * B_TW, B_UA = 4160, B_K = 4096, B_XA = 10240;
template <int R0, int R1, int R2, int R3>
DI void put4(u32 a, u32 b, u32 c, u32 d) {
    asm volatile("ds_write_addtid_b32 %0 offset:%4\n\tds_write_addtid_b32 %1 offset:%5\n\tds_write_addtid_b32 %2 offset:%6\n\tds_write_addtid_b32 %3 offset:%7"
                 :: "v"(a), "v"(b), "v"(c), "v"(d), "n"(R0 * B_TROW), "n"(R1 * B_TROW), "n"(R2 * B_TROW), "n"(R3 * B_TROW) : "memory");
}

DI void role_b(int bb, const u16* u, const u16* lar, const u16* aim, const u16* cre, const u16* cim,
               const u16* ldt, const u16* dsk, u16* out, char* ws) {
    const int tid = threadIdx.x, lane = tid & 63;
    const int w = __builtin_amdgcn_readfirstlane(tid >> 6);
    const int cg = bb % 7, bi = bb / 7;
    const int ch = w >> 1, hw = w & 1;
    const int c = cg * 64 + bi * 2 + ch;
    const int l15 = lane & 15, g = lane >> 4;
    rsrc_t rws = mk_rsrc(ws, WS_USED);
    STDECL ST(0);
    u32 po = lane * 4, ps = c * 512 + hw * 256;
    u32 p_lar = __builtin_amdgcn_raw_buffer_load_b32(mk_rsrc(lar, 448 * 512), po, ps, 0);
    u32 p_aim = __builtin_amdgcn_raw_buffer_load_b32(mk_rsrc(aim, 448 * 512), po, ps, 0);
    u32 p_cre = __builtin_amdgcn_raw_buffer_load_b32(mk_rsrc(cre, 448 * 512), po, ps, 0);
    u32 p_cim = __builtin_amdgcn_raw_buffer_load_b32(mk_rsrc(cim, 448 * 512), po, ps, 0);
    rsrc_t ru = mk_rsrc(u, 2048 * 448 * 2);
    u32 uo = (lane >> 3) * 896 + (lane & 7) * 16;
    u32 us = (bi * 64 + w * 16) * 896 + cg * 128;
    u32x4 t0, t1;
    asm volatile("buffer_load_dwordx4 %0, %2, %3, %4 offen\n\tbuffer_load_dwordx4 %1, %2, %3, %5 offen"
                 : "=&v"(t0), "=&v"(t1) : "v"(uo), "s"(ru), "s"(us), "s"(us + 8 * 896) : "memory");
    u32 ldt2 = sld(ldt + (c & ~1)), dsk2 = sld(dsk + (c & ~1)), ep = sld(ws + WS_EPOCH + (1 + cg) * 256), aep = sld(ws + WS_EPOCH);
    asm volatile("" : "+s"(ldt2), "+s"(dsk2), "+s"(ep), "+s"(aep));
    const u32 tag = mk_tag(ep), atag = mk_tag(aep);
    SB();
    const u32 tb = w * B_TW;
    asm volatile("s_mov_b32 m0, %0" :: "s"(tb) : "memory");
    const float dt = rbf(expf(ch ? bhi(ldt2) : blo(ldt2)));
    const float dskip = ch ? bhi(dsk2) : blo(dsk2);
    const int sl = lane & 31;
    const u32 hrow = sl - (u32)(((sl < 16 ? 0x3376111111100000ull : 0x5555555554444333ull) >> (4 * (sl & 15))) & 7);
    const lptr trl = lp(tb + sl * B_TROW + (lane >> 5) * 16);
    const lptr trh = lp(tb + (32 + hrow) * B_TROW + (lane >> 5) * 16);
    if (tid < 8) st<u32x4>(lp(B_U + (tid >> 1) * B_UA + (tid & 1) * 16), 0, (u32x4){0u, 0u, 0u, 0u});
    asm volatile("" : "+v"(p_lar), "+v"(p_aim), "+v"(p_cre), "+v"(p_cim) :: "memory");
    clear_issue(rws, bb, w, lane);
    ST(1);
    f32x16 kd;
#pragma unroll
    for (int i = 0; i < 16; ++i) kd[i] = 0.f;
    u32x4 gv[4];
#pragma unroll
    for (int k = 0; k < 4; ++k) asm volatile("" : "=v"(gv[k]));
    const u32 gs = WS_UT + (cg * 32 + bi) * 16384 + w * 4096;
#pragma unroll 1
    for (int rnd = 0; rnd < 2; ++rnd) {
        float e2 = -rbf(expf(blo(p_lar))) * dt * 1.44269504088896f;
        float rev = blo(p_aim) * dt * 0.159154943091895f;
        f32x2 C = {2.f * dt * blo(p_cre), -2.f * dt * blo(p_cim)};
        p_lar >>= 16; p_aim >>= 16; p_cre >>= 16; p_cim >>= 16;
        Cm w1 = mult(cexp(e2, -rev, 1.f)), w8 = mult(cexp(e2, rev, 8.f)), w256 = mult(cexp(e2, -rev, 256.f));
        Cm w2 = mult(cmul(w1.w, w1)), w4 = mult(cmul(w2.w, w2));
        Cm w8c = mult((f32x2){w8.w.x, -w8.w.y});
        f32x2 L = w8.w, H = C, G = C;
        u32 a, b, cc, d;
#define LN (L = cmul(L, w8), pkc(L))
#define HN(m) (H = cmul(H, m), pkc(H))
#define GN (G = cmul(G, w256), H = G, pkc(G))
        a = 0x00003f80u; b = pkc(L); cc = pkc(H); d = HN(w1);  put4<0, 1, 32, 33>(a, b, cc, d);
        a = LN; b = HN(w1); cc = LN; d = HN(w1);               put4<2, 34, 3, 35>(a, b, cc, d);
        a = LN; b = HN(w1); cc = LN; d = HN(w1);               put4<4, 39, 5, 40>(a, b, cc, d);
        a = LN; b = HN(w1); cc = LN; d = HN(w1);               put4<6, 41, 7, 42>(a, b, cc, d);
        a = LN; b = GN; cc = LN; d = HN(w2);                   put4<8, 36, 9, 37>(a, b, cc, d);
        a = LN; b = HN(w2); cc = LN; d = HN(w2);               put4<10, 38, 11, 43>(a, b, cc, d);
        a = LN; b = HN(w2); cc = LN; d = GN;                   put4<12, 44, 13, 45>(a, b, cc, d);
        if (rnd == 0) {
            clear_flag(rws, bb, w, lane, atag);
            asm volatile("" : "+v"(t0), "+v"(t1) :: "memory");
            const u32 sb = tb + 46 * B_TROW;
            lptr p = lp(sb + (lane >> 3) * 160 + (lane & 7) * 16);
            lptr ra = lp(sb + (4 * g + (l15 >> 2)) * 160 + (l15 & 3) * 8);
            u32 so = WS_UT + (u32)(cg * 32 + (l15 >> 1)) * 16384 + bi * 512 + w * 128 + (l15 & 1) * 64 + g * 16;
            st<u32x4>(p, 0, t0);
            st<u32x4>(p, 8 * 160, t1);
            u32x2 d4[4];
#pragma unroll
            for (int q = 0; q < 4; ++q) d4[q] = ldtr(ra, q * 32);
            SB();
#pragma unroll
            for (int q = 0; q < 4; ++q) st128(rws, so + q * 8 * 16384, (u32x4){d4[q].x, tag, d4[q].y, tag}, 1);
            ST(14);
        }
        a = LN; b = HN(w4); cc = LN; d = HN(w4);               put4<14, 46, 15, 47>(a, b, cc, d);
        a = LN; b = GN; cc = LN; d = HN(w4);                   put4<16, 48, 17, 49>(a, b, cc, d);
        a = LN; b = HN(w4); cc = LN; d = GN;                   put4<18, 50, 19, 51>(a, b, cc, d);
        a = LN; b = HN(w8c); cc = LN; d = GN;                  put4<20, 52, 21, 53>(a, b, cc, d);
        a = LN; b = HN(w8c); cc = LN; d = GN;                  put4<22, 54, 23, 55>(a, b, cc, d);
        a = LN; b = HN(w8c); cc = LN; d = GN;                  put4<24, 56, 25, 57>(a, b, cc, d);
        a = LN; b = HN(w8c); cc = LN; d = LN;                  put4<26, 58, 27, 28>(a, b, cc, d);
        a = LN; b = LN; cc = LN;                               put4<29, 30, 31, 31>(a, b, cc, cc);
#undef LN
#undef HN
#undef GN
        if (rnd) ST(11); else ST(10);
        u32x4 lo[8], hi[8];
#pragma unroll
        for (int s = 0; s < 8; ++s) { hi[s] = ld<u32x4>(trh, s * 32); lo[s] = ld<u32x4>(trl, s * 32); }
        SB();
        gl4_if(rnd, rws, lane * 16, gs, gv[0], gv[1], gv[2], gv[3]);
#pragma unroll
        for (int s = 0; s < 8; ++s) kd = MFMA32(hi[s], lo[s], kd);
        cbar();
    }
    ST(2);
    {
        const lptr xw = lp(tb + lane * 16);
        f32x4 s0, s1;
#pragma unroll
        for (int i = 0; i < 4; ++i) { s0[i] = hw ? kd[i] : kd[8 + i]; s1[i] = hw ? kd[4 + i] : kd[12 + i]; }
        st<f32x4>(xw, 0, s0);
        st<f32x4>(xw, 1024, s1);
    }
    {
        const lptr ua = lp(B_U + ((lane >> 2) & 1) * 2 * B_UA + 30 + w * 1024 + (lane >> 3) * 32 + (lane & 3) * 8);
        int sp = gwait<4>(rws, lane * 16, gs, 0, tag, gv);
        STV(12, sp);
#pragma unroll
        for (int k = 0; k < 4; ++k) {
            st<u32x2>(ua, 2 + k * 256, (u32x2){gv[k].x, gv[k].z});
            st<u16>(ua, B_UA + k * 256, (u16)gv[k].x);
            st<u32>(ua, B_UA + 2 + k * 256, __builtin_amdgcn_alignbit(gv[k].z, gv[k].x, 16));
            st<u16>(ua, B_UA + 6 + k * 256, (u16)(gv[k].z >> 16));
        }
    }
    bar();
    ST(3);
    u32x4 ua[2][8], kf[2][8];
    lptr up[4];
    {
        const u32 ab = B_U + (ch * 2 + (~l15 & 1)) * B_UA + 32 + (64 * hw + 4 * g + ((l15 - 15) >> 1)) * 4;
#pragma unroll
        for (int i2 = 0; i2 < 4; ++i2) up[i2] = lp(ab + i2 * 1024);
    }
#define LDUA(buf, rr) _Pragma("unroll") for (int i = 0; i < 8; ++i) _Pragma("unroll") for (int q = 0; q < 4; ++q) \
        ua[buf][i][q] = ld<u32>(up[i >> 1], ((i & 1) * 128 + (rr) * 16 + q) * 4);
    {
        const lptr xr = lp((w ^ 1) * B_TW + lane * 16);
        f32x4 p0 = ld<f32x4>(xr, 0), p1 = ld<f32x4>(xr, 1024);
        LDUA(0, 0)
        SB();
        const int j = lane & 31, h = lane >> 5;
        const u32 kb = ch * 2 * B_TW + B_K;
        if (hw == 0) {
#pragma unroll
            for (int i = 0; i < 4; ++i) { p0[i] += kd[i]; p1[i] += kd[4 + i]; }
            st<u32x4>(lp(kb + 4080 - 16 * lane), 0, (u32x4){pk2(p1[3], p1[2]), pk2(p1[1], p1[0]), pk2(p0[3], p0[2]), pk2(p0[1], p0[0])});
            if (lane < 30) st<u32x4>(lp(kb + 4096 + 16 * lane), 0, (u32x4){0u, 0u, 0u, 0u});
        } else {
#pragma unroll
            for (int i = 0; i < 4; ++i) { p0[i] += kd[8 + i]; p1[i] += kd[12 + i]; }
            st<u32x4>(lp(kb + 4080 - 1024 - 16 * lane), 0, (u32x4){pk2(p0[2], p0[2]), pk2(p0[1], p0[1]), pk2(p0[1], p0[0]), pk2(p0[0], p0[0])});
            const lptr k3 = lp(kb + 4080 - 2048 - 512 - 16 * j - 1024 * h);
            u32 aa = pk2(p1[0], p1[0]), bb2 = pk2(p1[2], p1[2]);
            st<u32x4>(k3, 512, (u32x4){pk2(p1[1], p1[1]), pk2(p1[1], (j & 1) ? p1[1] : p1[0]), aa, aa});
            st<u32x4>(k3, 0, (u32x4){pk2(p1[3], p1[3]), pk2(p1[3], (j & 1) ? p1[3] : p1[2]), bb2, bb2});
        }
    }
    bar();
    ST(4);
    f32x4 acc[8];
    f32x4 sk[4];
#pragma unroll
    for (int i = 0; i < 8; ++i) acc[i] = (f32x4){0.f, 0.f, 0.f, 0.f};
    {
        const lptr kp = lp(ch * 2 * B_TW + B_K + (2032 - 1792 + 128 * hw - 16 * l15 + 8 * g) * 2);
        const lptr sp = lp(B_U + ch * 2 * B_UA + 32 + hw * 2048 + 32 * l15 + 8 * g);
        u32x2 su[4];
#pragma unroll
        for (int q = 0; q < 8; ++q) kf[0][q] = ld<u32x4>(kp, (7 - q) * 512);
#pragma unroll
        for (int rr = 0; rr < 4; ++rr) {
            if (rr < 3) {
                LDUA((rr + 1) & 1, rr + 1)
#pragma unroll
                for (int q = 0; q < 8; ++q) kf[(rr + 1) & 1][q] = ld<u32x4>(kp, (rr + 1) * 64 + (7 - q) * 512);
            } else {
#pragma unroll
                for (int k = 0; k < 4; ++k) su[k] = ld<u32x2>(sp, k * 512);
            }
#pragma unroll
            for (int q = 0; q < 8; ++q)
#pragma unroll
                for (int i = 0; i + q < 8; ++i) acc[i + q] = MFMA16(ua[rr & 1][i], kf[rr & 1][q], acc[i + q]);
            if (rr < 3) {
#pragma unroll
                for (int n = 0; n < 12; ++n) { __builtin_amdgcn_sched_group_barrier(0x008, 2, 0); __builtin_amdgcn_sched_group_barrier(0x100, 2, 0); }
            } else {
#pragma unroll
                for (int k = 0; k < 4; ++k) {
                    u32 a = pk2(blo(su[k].x) * dskip, bhi(su[k].x) * dskip), b = pk2(blo(su[k].y) * dskip, bhi(su[k].y) * dskip);
                    sk[k] = (f32x4){blo(a), bhi(a), blo(b), bhi(b)};
                }
            }
            SB();
        }
    }
#undef LDUA
    ST(5);
    {
        const lptr xw = lp(tb + B_XA + lane * 16), xr = lp((w ^ 1) * B_TW + B_XA + lane * 16);
        if (hw) {
#pragma unroll
            for (int k = 0; k < 4; ++k) { st<f32x4>(xw, k * 1024, acc[k]); acc[k] = acc[4 + k]; }
            cbar();
        } else {
#pragma unroll
            for (int k = 0; k < 4; ++k) st<f32x4>(xw, k * 1024, acc[4 + k]);
        }
        const u32 so = WS_YT + (u32)(cg * 32 + hw * 16 + (l15 >> 2)) * 16384 + bi * 512 + ch * 256 + ((l15 & 3) * 4 + g) * 16;
        bar();
        f32x4 o[4];
#pragma unroll
        for (int k = 0; k < 4; ++k) o[k] = ld<f32x4>(xr, k * 1024);
        SB();
#pragma unroll
        for (int k = 0; k < 4; ++k) {
            f32x4 y = acc[k] + o[k];
            u32 y01 = pk2(y[0], y[1]), y23 = pk2(y[2], y[3]);
            u32 o01 = pk2(blo(y01) + sk[k][0], bhi(y01) + sk[k][1]), o23 = pk2(blo(y23) + sk[k][2], bhi(y23) + sk[k][3]);
            st128(rws, so + k * 65536, (u32x4){o01, tag, o23, tag}, 1);
        }
    }
    ST(6);
    {
        const u32 ys = WS_YT + (cg * 32 + bi) * 16384 + w * 4096;
        gload<4>(rws, lane * 16, ys, 0, gv);
        const lptr sa = lp(B_U + (w * 16 + g) * 160 + l15 * 8);
        int sp = gwait<4>(rws, lane * 16, ys, 0, tag, gv);
        STV(13, sp);
#pragma unroll
        for (int k = 0; k < 4; ++k) st<u32x2>(sa, k * 4 * 160, (u32x2){gv[k].x, gv[k].z});
        bar();
        ST(7);
        rsrc_t ro = mk_rsrc(out, 2048 * 896 * 2);
        const lptr ra = lp(B_U + (8 * g + (l15 >> 2)) * 160 + w * 32 + (l15 & 3) * 8);
        const u32 oo = (u32)(bi * 64 + w * 16 + l15) * 1792 + 896 + cg * 128 + g * 16;
        u32x2 d[4];
#pragma unroll
        for (int k = 0; k < 4; ++k) d[k] = ldtr(ra, (k >> 1) * 32 * 160 + (k & 1) * 4 * 160);
        SB();
#pragma unroll
        for (int k = 0; k < 2; ++k) st128(ro, oo + k * 64, (u32x4){d[2 * k].x, d[2 * k].y, d[2 * k + 1].x, d[2 * k + 1].y}, 0);
        if (bi == 0 && tid == 0) __builtin_amdgcn_raw_buffer_store_b32(tag, rws, 0, WS_EPOCH + (1 + cg) * 256, 16);
    }
    ST(8);
    STEND;
}

constexpr u32 A_KV = 57344, A_KVM = 12288, A_KVF = 49152;
#define A_D1 75
#define A_D2 35
#define A_DQ 110
#define A_NPRE 6
DI void dma16(rsrc_t rs, u32 voff, u32 soff, u32 lbase, int loff) {
    asm volatile("s_add_u32 m0, %3, %4\n\ts_nop 0\n\tbuffer_load_dwordx4 %0, %1, %2 offen lds"
                 :: "v"(voff), "s"(rs), "s"(soff), "s"(lbase), "n"(loff) : "memory", "scc");
}
template <int OFF> DI void ldw(u32x2& d, rsrc_t rs, u32 voff, u32 soff) {
    asm volatile("buffer_load_dwordx2 %0, %1, %2, %3 offen offset:%4" : "=v"(d) : "v"(voff), "s"(rs), "s"(soff), "n"(OFF) : "memory");
}
template <int OFF> DI void gl1(u32x4& d, rsrc_t rs, u32 voff, u32 soff) {
    asm volatile("buffer_load_dwordx4 %0, %1, %2, %3 offen offset:%4 sc1" : "+v"(d) : "v"(voff), "s"(rs), "s"(soff), "n"(OFF) : "memory");
}
template <int OFF> DI void gs1(u32x4 d, rsrc_t rs, u32 voff) {
    asm volatile("buffer_store_dwordx4 %0, %1, %2, 0 offen offset:%3 sc1\n\ts_nop 1" :: "v"(d), "v"(voff), "s"(rs), "n"(OFF) : "memory");
}
template <int N> DI void vmw() { asm volatile("s_waitcnt vmcnt(%0)" :: "n"(N) : "memory"); }
template <int N> DI void vmw8(u32x2 (&a)[8]) {
    asm volatile("s_waitcnt vmcnt(%8)" : "+v"(a[0]), "+v"(a[1]), "+v"(a[2]), "+v"(a[3]), "+v"(a[4]), "+v"(a[5]), "+v"(a[6]), "+v"(a[7]) : "n"(N) : "memory");
}
DI u32 now() { return (u32)__builtin_amdgcn_s_memrealtime(); }
DI void until(u32 t) {
#pragma unroll 1
    for (int i = 0; i < 4096 && (int)(now() - t) < 0; ++i) __builtin_amdgcn_s_sleep(1);
}
DI u32 max3(u32 a, u32 b, u32 c) { u32 m = a > b ? a : b; return m > c ? m : c; }
DI u32 mx4(u32x4 v, u32 m) { return max3(max3(m, v.x, v.y), v.z, v.w); }
DI u32 nos(u32 v) { return v < SENT - 1 ? v : SENT - 1; }
DI float phi(float t) {
    float e = __builtin_amdgcn_exp2f(__builtin_fmaf(t, 1.44269504088896f, -1.44269504088896e-6f));
    return __builtin_amdgcn_fmed3f(e, 0.f, 1.f) + __builtin_fmaxf(t, 1e-6f);
}
DI void gl4_if64(u64 cond, rsrc_t rs, u32 voff, u32 soff, u32x4& a, u32x4& b, u32x4& c, u32x4& d) {
    asm volatile("s_cmp_eq_u64 %7, 0\n\ts_cbranch_scc1 1f\n\t"
                 "buffer_load_dwordx4 %0, %4, %5, %6 offen sc1\n\tbuffer_load_dwordx4 %1, %4, %5, %6 offen offset:1024 sc1\n\t"
                 "buffer_load_dwordx4 %2, %4, %5, %6 offen offset:2048 sc1\n\tbuffer_load_dwordx4 %3, %4, %5, %6 offen offset:3072 sc1\n1:"
                 : "+v"(a), "+v"(b), "+v"(c), "+v"(d) : "v"(voff), "s"(rs), "s"(soff), "s"(cond) : "memory", "scc");
}
template <int N, int YOUNG>
DI int swait(rsrc_t rs, u32 voff, u32 soff, u32x4 (&v)[N]) {
    vmw<YOUNG>();
    int spin = 0;
#pragma unroll 1
    for (; spin < SPIN_CAP; ++spin) {
        u32 m[N / 4], any = 0;
#pragma unroll
        for (int k = 0; k < N; k += 4) {
            tie(v[k], v[k + 1], v[k + 2], v[k + 3]);
            m[k / 4] = mx4(v[k + 3], mx4(v[k + 2], mx4(v[k + 1], mx4(v[k], 0))));
            any = any > m[k / 4] ? any : m[k / 4];
        }
        if (__builtin_amdgcn_ballot_w64(any == SENT) == 0) break;
#pragma unroll
        for (int k = 0; k < N; k += 4)
            gl4_if64(__builtin_amdgcn_ballot_w64(m[k / 4] == SENT), rs, voff, soff + k * 1024, v[k], v[k + 1], v[k + 2], v[k + 3]);
        vmw<0>();
    }
#pragma unroll
    for (int k = 0; k < N; k += 4) tie(v[k], v[k + 1], v[k + 2], v[k + 3]);
    return spin;
}

struct It { int kind, idx, when; };
#define A_PLAN {0,0,-1},{1,0,-1},{0,1,-1},{1,1,-1},{1,2,-1},{0,2,-1}, {0,3,0},{1,3,0},{0,4,0},{0,5,0},{0,6,0}, {1,4,1},{1,5,1}, {1,6,2},{1,7,3},{1,8,4},{1,9,5},{1,10,6},{1,11,7},{1,12,8},{1,13,9},{3,0,10}
#define A_BAR2 3
constexpr It PLAN[] = {A_PLAN};
constexpr int NPLAN = sizeof(PLAN) / sizeof(It);
constexpr int nops(int k) { return k == 0 ? 2 : (k == 1 ? 8 : (k == 2 ? 4 : 1)); }
constexpr int younger(int kind, int idx, int step) {
    int n = 0; bool seen = false;
    for (int i = 0; i < NPLAN; ++i) {
        if (seen && PLAN[i].when < step) n += nops(PLAN[i].kind);
        if (PLAN[i].kind == kind && PLAN[i].idx == idx) seen = true;
    }
    return n > 63 ? 63 : n;
}
constexpr int cmin(int a, int b) { return a < b ? a : b; }
constexpr int opcount(int step) { int n = 0; for (int i = 0; i < NPLAN; ++i) if (PLAN[i].when == step) n += nops(PLAN[i].kind); return n; }
constexpr int opitem(int step, int o) { for (int i = 0; i < NPLAN; ++i) if (PLAN[i].when == step) { if (o < nops(PLAN[i].kind)) return i; o -= nops(PLAN[i].kind); } return -1; }
constexpr int opsub(int step, int o) { for (int i = 0; i < NPLAN; ++i) if (PLAN[i].when == step) { if (o < nops(PLAN[i].kind)) return o; o -= nops(PLAN[i].kind); } return -1; }

struct ACtx {
    rsrc_t rx, rw, rws;
    u32 xo[2], xs, lb, wvo[4], wso, col[7], fo;
    u32x4 fl;
    u32x2 wv[14][8];
};
template <int STEP, int O> DI void a_op(ACtx& c) {
    constexpr int it = opitem(STEP, O), sub = opsub(STEP, O);
    if constexpr (it >= 0) {
        constexpr int kind = PLAN[it].kind, idx = PLAN[it].idx;
        if constexpr (kind == 0) dma16(c.rx, c.xo[sub & 1], c.xs + c.col[idx] * 128, c.lb, idx * 8192 + sub * 1024);
        if constexpr (kind == 1) ldw<(sub & 1) * 2688>(c.wv[idx][sub], c.rw, c.wvo[sub >> 1], c.wso + (c.col[idx >> 1] * 64 + (idx & 1) * 32) * 2688);
        if constexpr (kind == 3) gl1<0>(c.fl, c.rws, c.fo, WS_CFLAG);
    }
}
template <int STEP, int O, int END> DI void a_ops(ACtx& c) { if constexpr (O < END) { a_op<STEP, O>(c); a_ops<STEP, O + 1, END>(c); } }
DI u32 wperm(const u32x2 (&v)[8], int jj, int p) {
    u32 hi = (jj & 2) ? v[2 * p + 1].y : v[2 * p + 1].x, lo = (jj & 2) ? v[2 * p].y : v[2 * p].x;
    u32 r = (jj & 1) ? PHI_(hi, lo) : PLO(hi, lo);
    asm volatile("" : "+v"(r));
    return r;
}
template <int S, int K>
DI void a_slot(ACtx& c, f32x4 (&acc)[4][4], u32x4 (&wf)[4], u32x4 (&xf)[2][4], lptr xp0, lptr xp1) {
    if constexpr (K < 16) {
        constexpr int jj = K >> 2, rt = K & 3, n = opcount(S), per = (n + 11) / 12;
        acc[rt][jj] = MFMA16(xf[S & 1][rt], wf[jj], acc[rt][jj]);
        asm volatile("" : "+v"(acc[rt][jj]));
        if constexpr (K == 4 && S < 13) vmw8<younger(1, S + 1, S)>(c.wv[S + 1]);
        if constexpr (K < 4) {
            wf[3][K] = wperm(c.wv[S], 3, K);
            if constexpr (S < 13) xf[(S + 1) & 1][K] = ld<u32x4>((S & 1) ? xp0 : xp1, ((S + 1) >> 1) * 8192 + K * 2048);
        } else {
            if constexpr (S < 13) wf[jj - 1][rt] = wperm(c.wv[S + 1], jj - 1, rt);
            a_ops<S, (K - 4) * per, cmin(n, (K - 3) * per)>(c);
        }
        SB();
        a_slot<S, K + 1>(c, acc, wf, xf, xp0, xp1);
    }
}
template <int S>
DI void a_steps(ACtx& c, f32x4 (&acc)[4][4], u32x4 (&wf)[4], u32x4 (&xf)[2][4], lptr xp0, lptr xp1) {
    if constexpr (S < 14) {
        if constexpr (S == A_BAR2) {
            asm volatile("s_waitcnt vmcnt(%0)\n\ts_barrier" :: "n"(younger(0, 6, S)) : "memory");
        }
        a_slot<S, 0>(c, acc, wf, xf, xp0, xp1);
        a_steps<S + 1>(c, acc, wf, xf, xp0, xp1);
    }
}

DI void role_a(int b, const u16* x, const u16* wqkv, const u16* wo, u16* out, char* ws) {
    const int tid = threadIdx.x, lane = tid & 63;
    const int w = __builtin_amdgcn_readfirstlane(tid >> 6);
    const int l15 = lane & 15, g = lane >> 4;
    const int j = (b >> 3) & 7, rgl = b >> 6, rg = (b & 7) * 4 + rgl;
    const int line = j < 6 ? (w < 2 ? 7 : 12) + j + w : (j - 6) * 4 + w;
    const bool gemm = !(j == 7 && w == 3);
    STDECL ST(0);
    ACtx c;
    c.rws = mk_rsrc(ws, WS_USED);
    const rsrc_t rws = c.rws;
    c.rx = mk_rsrc(x, 2048 * 448 * 2);
    c.rw = mk_rsrc(wqkv, 448 * 2688);
#pragma unroll
    for (int cc = 0; cc < 7; ++cc) { u32 q = cc + 2 * rgl; c.col[cc] = q >= 7 ? q - 7 : q; }
#pragma unroll
    for (int rb = 0; rb < 2; ++rb) c.xo[rb] = (rb * 8 + (lane >> 3)) * 896 + (((lane & 7) ^ ((rb * 4 + g) & 7)) * 16);
    c.xs = (rg * 64 + w * 16) * 896;
    c.lb = w * 2048;
#pragma unroll
    for (int p = 0; p < 4; ++p) c.wvo[p] = (8 * g + 2 * p) * 2688 + l15 * 8 + (gemm ? 0 : OOB);
    c.wso = line * 128;
    a_ops<-1, 0, opcount(-1)>(c);
    SB();
    const u32 tag = mk_tag(sload(ws + WS_EPOCH));
    c.fo = tid * 16;
    asm volatile("" : "=v"(c.fl));
    f32x4 acc[4][4];
    {
        const float ini = (j < 6 && w >= 2) ? 0.f : 1e-6f;
#pragma unroll
        for (int rt = 0; rt < 4; ++rt)
#pragma unroll
            for (int jj = 0; jj < 4; ++jj) acc[rt][jj] = (f32x4){ini, ini, ini, ini};
        const lptr xp0 = lp(l15 * 128 + ((g ^ (l15 >> 1)) * 16)), xp1 = lp(l15 * 128 + (((4 + g) ^ (l15 >> 1)) * 16));
        u32x4 wf[4], xf[2][4];
        SB();
        vmw8<younger(1, 0, 0)>(c.wv[0]);
#pragma unroll
        for (int jj = 0; jj < 3; ++jj)
#pragma unroll
            for (int p = 0; p < 4; ++p) wf[jj][p] = wperm(c.wv[0], jj, p);
        SB();
        asm volatile("s_waitcnt vmcnt(%0)\n\ts_barrier" :: "n"(younger(0, (A_BAR2 + 0) >> 1, 0)) : "memory");
        ST(1);
#pragma unroll
        for (int rt = 0; rt < 4; ++rt) xf[0][rt] = ld<u32x4>(xp0, rt * 2048);
        SB();
        a_steps<0>(c, acc, wf, xf, xp0, xp1);
    }
    ST(2);
    {
        int spin = 0;
#pragma unroll 1
        for (; spin < SPIN_CAP; ++spin) {
            asm volatile("s_waitcnt vmcnt(0)" : "+v"(c.fl) :: "memory");
            if (__builtin_amdgcn_ballot_w64(((c.fl.x ^ tag) | (c.fl.y ^ tag) | (c.fl.z ^ tag) | (c.fl.w ^ tag)) != 0) == 0) break;
            gl1<0>(c.fl, rws, c.fo, WS_CFLAG);
        }
        STV(11, spin);
    }
    const int cga = j < 6 ? j : 6;
    const int nh = (j == 3 || j == 4) ? 2 : 3;
    u32 t0;
    if (j < 6) {
        const int h0 = j < 4 ? 3 * j : 2 * j + 3;
        const int ir = (w & 1) * 16 + l15 - (7 * h0 - 16 * j);
        if (w < 2) {
#pragma unroll
            for (int rt = 0; rt < 4; ++rt)
#pragma unroll
                for (int jj = 0; jj < 4; ++jj)
#pragma unroll
                    for (int i = 0; i < 4; ++i) acc[rt][jj][i] = phi(acc[rt][jj][i]);
        }
        if ((u32)ir < 21u) {
            const lptr p = lp(A_KV + (w >> 1) * A_KVM + g * 1536 + (4 * ir + (ir >> 1)) * 16);
#pragma unroll
            for (int kc = 0; kc < 2; ++kc)
#pragma unroll
                for (int jj = 0; jj < 4; ++jj)
                    st<u32x4>(p, kc * 6144 + jj * 16, (u32x4){pk2(acc[2 * kc][jj][0], acc[2 * kc][jj][1]), pk2(acc[2 * kc][jj][2], acc[2 * kc][jj][3]),
                                                           pk2(acc[2 * kc + 1][jj][0], acc[2 * kc + 1][jj][1]), pk2(acc[2 * kc + 1][jj][2], acc[2 * kc + 1][jj][3])});
        }
        const int n = lane & 31, h2 = lane >> 5;
        const int sl = ((n >> 4) * 2 + ((n >> 2) & 1)) * 2 + ((n >> 3) & 1);
        const int ck = 28 * w + (sl ? 4 * sl - 4 : 0) + (n & 3), slot = (n >> 4) * 4 + (n & 3), cv = 28 * w + 7 * ((n >> 2) & 3) + slot;
        const lptr kp = lp(A_KV + h2 * 1536 + (ck + (ck >> 3)) * 16), vp = lp(A_KV + A_KVM + h2 * 1536 + (cv + (cv >> 3)) * 16);
        const u32 so = WS_PART + (u32)((h0 + w) * 16 + (n >> 4) * 8 + h2 * 2 + ((n >> 3) & 1)) * 4096 + rg * 128 + (n & 7) * 16;
        ST(3);
        bar();
        t0 = now();
        ST(4);
        if (w < nh) {
            u32x4 kf[4], vf[4];
#pragma unroll
            for (int s = 0; s < 4; ++s) { kf[s] = ld<u32x4>(kp, s * 3072); vf[s] = ld<u32x4>(vp, s * 3072); }
            f32x16 d;
#pragma unroll
            for (int i = 0; i < 16; ++i) d[i] = 0.f;
#pragma unroll
            for (int s = 0; s < 4; ++s) {
#pragma unroll
                for (int q = 0; q < 4; ++q) vf[s][q] = slot == 7 ? 0x3f803f80u : vf[s][q];
                d = MFMA32(kf[s], vf[s], d);
            }
            const u32 cap = h2 ? SENT - 1 : 0u;
            u32 e[8];
#pragma unroll
            for (int i = 0; i < 8; ++i) { u32 v = pk2(d[2 * i], d[2 * i + 1]), m = i < 2 ? cap : SENT - 1; e[i] = v < m ? v : m; }
            st128(rws, so, (u32x4){e[0], e[1], e[2], e[3]}, 1);
            st128(rws, so + 16384, (u32x4){e[4], e[5], e[6], e[7]}, 1);
        }
    } else {
        const int Q = line * 16 + l15, hd = (Q * 147) >> 10, sl = Q - 7 * hd + 1;
        const int hb = j == 6 ? 0 : 9;
        const lptr p = lp(((hd - hb) * 4 + (sl >> 1)) * 272 + (sl & 1) * 8 + g * 64);
        u32x2 pv[4][4];
        if (gemm) {
#pragma unroll
            for (int rt = 0; rt < 4; ++rt)
#pragma unroll
                for (int i = 0; i < 4; ++i)
                    pv[rt][i] = (u32x2){pk2(phi(acc[rt][0][i]), phi(acc[rt][1][i])), pk2(phi(acc[rt][2][i]), phi(acc[rt][3][i]))};
        }
        ST(3);
        bar();
        if (gemm) {
#pragma unroll
            for (int rt = 0; rt < 4; ++rt)
#pragma unroll
                for (int i = 0; i < 4; ++i) st<u32x2>(p, rt * 10880 + i * 16, pv[rt][i]);
        }
        bar();
        t0 = now();
        ST(4);
        const lptr rp = lp(w * 10880 + g * 272 + l15 * 16);
        const u32 msk = g ? ~0u : 0u;
        const u32 so = WS_PQ + rg * 65536 + w * 16384 + hb * 1024 + lane * 16;
        u32x4 f[10];
#pragma unroll
        for (int h = 0; h < 10; ++h) f[h] = ld<u32x4>(rp, h * 1088);
#pragma unroll
        for (int h = 0; h < 10; ++h) {
            f[h].x &= msk; f[h].y &= msk;
            u32 skip = h == 0 ? ((j == 7 && g == 0) ? OOB : 0) : (h == 9 ? (g ? OOB : 0) : 0);
            if (h < 7 || j == 6) st128(rws, so + h * 1024 + skip, f[h], 1);
        }
        if (j == 6) { ST(5); STEND; return; }
        bar();
    }
    ST(5);
    {
        rsrc_t ro = mk_rsrc(wo, 448 * 896);
        const u32 i = lane >> 3, ch = (lane & 7) ^ ((((i >> 1) & 1) | ((w & 1) << 1)) << 1);
        const u32 so = 7 * w * 896 + cga * 128;
#pragma unroll
        for (int n = 0; n < 7; ++n) {
            const u32 f = 8 * n + i, vo = (f + 21 * ((f * 37) >> 8)) * 896 + ch * 16;
            dma16(ro, vo, so, w * 1024, n * 4096);
            dma16(ro, vo, so + 224 * 896, w * 1024, (7 + n) * 4096);
        }
    }
    u32x4 pq[16], gv[8];
#pragma unroll
    for (int k = 0; k < 16; ++k) asm volatile("" : "=v"(pq[k]));
#pragma unroll
    for (int k = 0; k < 8; ++k) asm volatile("" : "=v"(gv[k]));
    const u32 vo = lane * 16, qs = WS_PQ + rg * 65536 + w * 16384;
    if (j < 6 && w >= nh) {
        const u32 sg = rg * 8 + (w == 3 ? j : j + 3);
        u32x4 sel;
#pragma unroll
        for (int q = 0; q < 4; ++q) sel[q] = ((l15 >> 1) & 3) == q ? ((l15 & 1) ? 0x3f800000u : 0x00003f80u) : 0u;
        u32x4 r[4];
#pragma unroll
        for (int k = 0; k < 4; ++k) asm volatile("" : "=v"(r[k]));
        until(t0 + A_D1);
        gload<4>(rws, vo, WS_PART + sg * 4096, 0, r);
        int sp = swait<4, 0>(rws, vo, WS_PART + sg * 4096, r);
        STV(12, sp);
        f32x4 s = {0.f, 0.f, 0.f, 0.f};
#pragma unroll
        for (int k = 0; k < 4; ++k) s = MFMA16(sel, r[k], s);
#pragma unroll
        for (int q = 0; q < 4; ++q) s[q] += __uint_as_float(__builtin_amdgcn_update_dpp(0u, __float_as_uint(s[q]), 0x128, 0xf, 0xf, false));
        u32 a = nos(pk2(s[0], s[1])), bq = nos(pk2(s[2], s[3]));
        auto ra = __builtin_amdgcn_permlane16_swap(a, a, false, false);
        auto rb = __builtin_amdgcn_permlane16_swap(bq, bq, false, false);
        st128(rws, lane < 8 ? WS_KVG + sg * 128 + lane * 16 : OOB, (u32x4){ra[0], rb[0], ra[1], rb[1]}, 1);
        if (j == 0) __builtin_amdgcn_raw_buffer_store_b32(tag, rws, lane ? OOB : 0, WS_NOTE + rg * 256, 16);
        ST(6);
    } else {
        until(t0 + A_DQ);
    }
    gload<16>(rws, vo, qs, 4096, pq);
    if (j == 7) {
        int spin = 0;
        if (w == 0) {
#pragma unroll 1
            for (; spin < SPIN_CAP; ++spin) {
                u32 f;
                asm volatile("buffer_load_dword %0, off, %1, %2 sc1\n\ts_waitcnt vmcnt(0)" : "=v"(f) : "s"(rws), "s"(WS_NOTE + rg * 256) : "memory");
                if (__builtin_amdgcn_readfirstlane(f) == tag) break;
                __builtin_amdgcn_s_sleep(2);
            }
        }
        STV(12, spin);
        bar();
    } else {
        bar();
        until(now() + A_D2);
    }
    ST(7);
    const u32 ks = WS_KVG + w * 8192;
    gload<8>(rws, vo, ks, 4096, gv);
    asm volatile("s_waitcnt vmcnt(24)\n\ts_barrier" ::: "memory");
    lptr wp[4];
#pragma unroll
    for (int ct = 0; ct < 4; ++ct) wp[ct] = lp(g * 1024 + (l15 >> 2) * 128 + ((ct ^ (((l15 >> 3) & 1) | ((g & 1) << 1))) * 32) + (l15 & 3) * 8);
#define LDWO(s, ct) ({ u32x2 a_ = ldtr(wp[ct], (s) * 4096), b_ = ldtr(wp[ct], (s) * 4096 + 512); (u32x4){a_.x, a_.y, b_.x, b_.y}; })
    u32x4 wr[14][4];
#pragma unroll
    for (int s = 14 - A_NPRE; s < 14; ++s)
#pragma unroll
        for (int ct = 0; ct < 4; ++ct) wr[s][ct] = LDWO(s, ct);
    bar();
    {
        int sp = swait<16, 8>(rws, vo, qs, pq);
        STV(13, sp);
    }
    ST(8);
    const lptr fp = lp(lane * 16);
    {
        int sp = swait<8, 0>(rws, vo, ks, gv);
        STV(14, sp);
        const lptr p = lp(A_KVF + w * 8192 + lane * 16);
#pragma unroll
        for (int k = 0; k < 8; ++k) st<u32x4>(p, k * 1024, gv[k]);
    }
    bar();
    ST(9);
    {
        f32x4 oc[4];
#pragma unroll
        for (int ct = 0; ct < 4; ++ct) oc[ct] = (f32x4){0.f, 0.f, 0.f, 0.f};
        float ov[112];
        u32x4 kf[16][2];
        f32x4 n0[16], n1[16];
#define LDKF(h) { kf[h][0] = ld<u32x4>(fp, A_KVF + (h) * 2048); kf[h][1] = ld<u32x4>(fp, A_KVF + (h) * 2048 + 1024); }
#define NUM(h) { n0[h] = MFMA16(kf[h][0], pq[h], ((f32x4){0.f, 0.f, 0.f, 0.f})); n1[h] = MFMA16(kf[h][1], pq[h], ((f32x4){0.f, 0.f, 0.f, 1e-6f})); }
        LDKF(0) LDKF(1) LDKF(2)
        SB();
        NUM(0)
        SB();
#pragma unroll
        for (int h = 0; h < 16; ++h) {
            if (h + 3 < 16) LDKF(h + 3)
#pragma unroll
            for (int s = 0; s < 14 - A_NPRE; ++s)
                if (8 * s + 7 <= 7 * (h + 1) + 6 && 8 * s + 7 > 7 * (h + 1) - 1) {
#pragma unroll
                    for (int ct = 0; ct < 4; ++ct) wr[s][ct] = LDWO(s, ct);
                }
            if (h + 1 < 16) NUM(h + 1)
            float r = __builtin_amdgcn_rcpf(n1[h][3]);
#pragma unroll
            for (int i = 0; i < 4; ++i) ov[7 * h + i] = n0[h][i] * r;
#pragma unroll
            for (int i = 0; i < 3; ++i) ov[7 * h + 4 + i] = n1[h][i] * r;
#pragma unroll
            for (int s = 0; s < 14; ++s) {
                if (8 * s + 7 <= 7 * h + 6 && 8 * s + 7 > 7 * h - 1) {
                    u32x4 of = {pk2(ov[8 * s], ov[8 * s + 1]), pk2(ov[8 * s + 2], ov[8 * s + 3]), pk2(ov[8 * s + 4], ov[8 * s + 5]), pk2(ov[8 * s + 6], ov[8 * s + 7])};
#pragma unroll
                    for (int ct = 0; ct < 4; ++ct) oc[ct] = MFMA16(wr[s][ct], of, oc[ct]);
                }
            }
            SB();
        }
#undef LDKF
#undef NUM
#undef LDWO
        rsrc_t ro = mk_rsrc(out, 2048 * 896 * 2);
        const u32 oo = (u32)(rg * 64 + w * 16 + l15) * 1792 + cga * 128 + (g & 1) * 32 + (g >> 1) * 16;
#pragma unroll
        for (int a = 0; a < 2; ++a) {
            auto r0 = __builtin_amdgcn_permlane16_swap(pk2(oc[2 * a][0], oc[2 * a][1]), pk2(oc[2 * a + 1][0], oc[2 * a + 1][1]), false, false);
            auto r1 = __builtin_amdgcn_permlane16_swap(pk2(oc[2 * a][2], oc[2 * a][3]), pk2(oc[2 * a + 1][2], oc[2 * a + 1][3]), false, false);
            st128(ro, oo + a * 64, (u32x4){r0[0], r1[0], r0[1], r1[1]}, 0);
        }
        if (b == 0 && tid == 0) __builtin_amdgcn_raw_buffer_store_b32(tag, rws, 0, WS_EPOCH, 16);
    }
    ST(10);
    STEND;
}

__global__ __launch_bounds__(256, 2) void fused_kernel(const u16* x, const u16* wqkv, const u16* wo, const u16* u,
        const u16* lar, const u16* aim, const u16* cre, const u16* cim, const u16* ldt, const u16* dsk, u16* out, char* ws) {
    __shared__ __attribute__((aligned(16))) char smem[81920];
    asm volatile("" :: "v"((u32)(size_t)(LDSAS char*)smem) : "memory");
    const int b = blockIdx.x;
    if (b >= 256) {
        if (b < 256 + 224) role_b(b - 256, u, lar, aim, cre, cim, ldt, dsk, out, ws);
        else
        {
            rsrc_t rws = mk_rsrc(ws, WS_USED);
            const int w = __builtin_amdgcn_readfirstlane(threadIdx.x >> 6), lane = threadIdx.x & 63;
            clear_issue(rws, b - 256, w, lane);
            clear_flag(rws, b - 256, w, lane, mk_tag(sload(ws + WS_EPOCH)));
        }
    } else {
        role_a(b, x, wqkv, wo, out, ws);
    }
}

extern "C" void kernel_launch(void* const* d_in, const int* in_sizes, int n_in, void* d_out, int out_size,
                              void* d_ws, size_t ws_size, hipStream_t stream) {
    fused_kernel<<<512, 256, 0, stream>>>((const u16*)d_in[0], (const u16*)d_in[1], (const u16*)d_in[2], (const u16*)d_in[3],
        (const u16*)d_in[4], (const u16*)d_in[5], (const u16*)d_in[6], (const u16*)d_in[7], (const u16*)d_in[8],
        (const u16*)d_in[9], (u16*)d_out, (char*)d_ws);
}
```

```cpp
#define R_SLP 8
#define X_BA 0
#define X_BR 1
#define X_UC 6
#define Q_SLP 8
#define X_EPF 29440
#define ROTX ((u32)(0x420420420ull>>4*(t+(idx&3)))&7)
#define X_EPB 0
#define B_D3 170
#define A_D1 170
#define A_D2 330
#define X_BP 410
#include <hip/hip_runtime.h>

typedef unsigned short u16;
typedef unsigned int u32;
typedef unsigned long long u64;
typedef short bf16x8 __attribute__((ext_vector_type(8)));
typedef short s16x4 __attribute__((ext_vector_type(4)));
typedef float f32x2 __attribute__((ext_vector_type(2)));
typedef float f32x4 __attribute__((ext_vector_type(4)));
typedef float f32x16 __attribute__((ext_vector_type(16)));
typedef u32 u32x2 __attribute__((ext_vector_type(2)));
typedef u32 u32x4 __attribute__((ext_vector_type(4)));
typedef __bf16 bf2_t __attribute__((ext_vector_type(2)));
#define DI __device__ __forceinline__
#define LDSAS __attribute__((address_space(3)))
typedef __amdgpu_buffer_rsrc_t rsrc_t;

#define CODE_PF 0
#define SPM 288
#define WS_BASE 0
constexpr u32 WS_EPOCH = 0;
constexpr u32 WS_CFLAG = 64 << 10;
constexpr u32 WS_QF = 128 << 10;
constexpr u32 WS_H1 = 192 << 10;
constexpr u32 WS_H2 = 256 << 10;
constexpr u32 WS_UF = 320 << 10;
constexpr u32 WS_H3 = 384 << 10;
constexpr u32 WS_KVG = 448 << 10;
constexpr u32 WS_PART = 512 << 10;
constexpr u32 WS_UT = 2 << 20;
constexpr u32 WS_PQ = 4 << 20;
constexpr u32 WS_YT = 6 << 20;
#define PADK 1
constexpr u32 UT_S = 8192 + 384 * PADK, YT_S = 4096 + 128 * PADK, PT_S = 4096 + 384 * PADK;
constexpr u32 WS_USED = 16 << 20;
constexpr u32 OOB = 0x40000000u;
constexpr u32 SENT = 0xffffffffu;
constexpr int SPIN_CAP = 1 << 14;

#define SB() __builtin_amdgcn_sched_barrier(0)

DI u32 pk2(float lo, float hi) { f32x2 v = {lo, hi}; bf2_t b = __builtin_convertvector(v, bf2_t); return __builtin_bit_cast(u32, b); }
DI float blo(u32 v) { return __uint_as_float(v << 16); }
DI float bhi(u32 v) { return __uint_as_float(v & 0xffff0000u); }
DI float rbf(float f) { return blo(pk2(f, f)); }
DI rsrc_t mk_rsrc(const void* p, u32 bytes) { return __builtin_amdgcn_make_buffer_rsrc((void*)p, 0, bytes, 0x00020000); }
DI void cbar() { asm volatile("" ::: "memory"); }
DI void bar() { asm volatile("s_waitcnt lgkmcnt(0)\n\ts_barrier" ::: "memory"); }
DI u32 mk_tag(u32 e) { u32 t = (e * 0x9E3779B1u + 0x7F4A7C15u) & 0xbfffbfffu; return t == e ? t ^ 1u : t; }
typedef LDSAS char* lptr;
DI lptr lp(u32 a) { lptr p = (lptr)a; asm("" : "+v"(p)); return p; }
template <typename T> DI T ld(lptr p, int off) { return *(const LDSAS T*)(p + off); }
template <typename T> DI void st(lptr p, int off, T v) { *(LDSAS T*)(p + off) = v; }
DI u32x2 ldtr(lptr p, int off) { s16x4 r = __builtin_amdgcn_ds_read_tr16_b64_v4i16((LDSAS s16x4*)(p + off)); return __builtin_bit_cast(u32x2, r); }
DI u32 sload(const void* p) { u32 v = *(const __attribute__((address_space(4))) u32*)p; asm volatile("" : "+s"(v)); return v; }
DI u32 sld(const void* p) { return *(const __attribute__((address_space(4))) u32*)p; }
DI u32 opq(u32 v) { asm("" : "+v"(v)); return v; }
DI void st128(rsrc_t rs, u32 voff, u32x4 v, int aux_sc1) {
    if (aux_sc1) __builtin_amdgcn_raw_buffer_store_b128(v, rs, voff, 0, 16); else __builtin_amdgcn_raw_buffer_store_b128(v, rs, voff, 0, 0);
}
#define MFMA16(a, b, c) __builtin_amdgcn_mfma_f32_16x16x32_bf16(__builtin_bit_cast(bf16x8, a), __builtin_bit_cast(bf16x8, b), (c), 0, 0, 0)
#define MFMA32(a, b, c) __builtin_amdgcn_mfma_f32_32x32x16_bf16(__builtin_bit_cast(bf16x8, a), __builtin_bit_cast(bf16x8, b), (c), 0, 0, 0)
#define PLO(b, a) __builtin_amdgcn_perm(b, a, 0x05040100u)

DI void gl4(rsrc_t rs, u32 voff, u32 soff, u32x4& a, u32x4& b, u32x4& c, u32x4& d) {
    asm volatile("buffer_load_dwordx4 %0, %4, %5, %6 offen sc1\n\tbuffer_load_dwordx4 %1, %4, %5, %6 offen offset:1024 sc1\n\t"
                 "buffer_load_dwordx4 %2, %4, %5, %6 offen offset:2048 sc1\n\tbuffer_load_dwordx4 %3, %4, %5, %6 offen offset:3072 sc1"
                 : "+v"(a), "+v"(b), "+v"(c), "+v"(d) : "v"(voff), "s"(rs), "s"(soff) : "memory");
}
DI void gl4_if(u32 cond, rsrc_t rs, u32 voff, u32 soff, u32x4& a, u32x4& b, u32x4& c, u32x4& d) {
    asm volatile("s_cmp_eq_u32 %7, 0\n\ts_cbranch_scc1 1f\n\t"
                 "buffer_load_dwordx4 %0, %4, %5, %6 offen sc1\n\tbuffer_load_dwordx4 %1, %4, %5, %6 offen offset:1024 sc1\n\t"
                 "buffer_load_dwordx4 %2, %4, %5, %6 offen offset:2048 sc1\n\tbuffer_load_dwordx4 %3, %4, %5, %6 offen offset:3072 sc1\n1:"
                 : "+v"(a), "+v"(b), "+v"(c), "+v"(d) : "v"(voff), "s"(rs), "s"(soff), "s"(cond) : "memory", "scc");
}
DI void vm0(u32x4& a, u32x4& b, u32x4& c, u32x4& d) { asm volatile("s_waitcnt vmcnt(0)" : "+v"(a), "+v"(b), "+v"(c), "+v"(d) :: "memory"); }
DI void tie(u32x4& a, u32x4& b, u32x4& c, u32x4& d) { asm volatile("" : "+v"(a), "+v"(b), "+v"(c), "+v"(d) :: "memory"); }
template <int N>
DI void gload(rsrc_t rs, u32 voff, u32 soff, u32 gstride, u32x4 (&v)[N]) {
#pragma unroll
    for (int k = 0; k < N; k += 4) gl4(rs, voff, soff + (k / 4) * gstride, v[k], v[k + 1], v[k + 2], v[k + 3]);
}
template <int N>
DI int gwait(rsrc_t rs, u32 voff, u32 soff, u32 gstride, u32 tag, u32x4 (&v)[N]) {
    int spin = 0;
#pragma unroll 1
    for (; spin < SPIN_CAP; ++spin) {
        vm0(v[0], v[1], v[2], v[3]);
#pragma unroll
        for (int k = 4; k < N; k += 4) tie(v[k], v[k + 1], v[k + 2], v[k + 3]);
        u32 bad = 0;
#pragma unroll
        for (int k = 0; k < N; ++k) bad |= (v[k].y ^ tag) | (v[k].w ^ tag);
        if (__builtin_amdgcn_ballot_w64(bad != 0) == 0) break;
        gload<N>(rs, voff, soff, gstride, v);
    }
    vm0(v[0], v[1], v[2], v[3]);
#pragma unroll
    for (int k = 4; k < N; k += 4) tie(v[k], v[k + 1], v[k + 2], v[k + 3]);
    return spin;
}

#define PIN(x) asm volatile("" : "+v"(x))
#define NOP() asm volatile("s_nop 0" ::: "memory")
#define BAR() asm volatile("s_barrier" ::: "memory")
#define M0(b, o) asm volatile("s_add_u32 m0, %0, %1" :: "s"(b), "n"(o) : "memory", "scc")
template <int OFF> DI void dmao(rsrc_t rs, u32 voff, u32 soff) {
    asm volatile("buffer_load_dwordx4 %0, %1, %2 offen offset:%3 lds" :: "v"(voff), "s"(rs), "s"(soff), "n"(OFF) : "memory");
}
template <int OFF> DI void gl1(u32x4& d, rsrc_t rs, u32 voff, u32 soff) {
    asm volatile("buffer_load_dwordx4 %0, %1, %2, %3 offen offset:%4 sc1" : "+v"(d) : "v"(voff), "s"(rs), "s"(soff), "n"(OFF) : "memory");
}
template <int N> DI void vmw() { asm volatile("s_waitcnt vmcnt(%0)" :: "n"(N) : "memory"); }
DI u32 now() { return (u32)__builtin_amdgcn_s_memrealtime(); }
DI void until(u32 t) {
#pragma unroll 1
    for (int i = 0; i < 1024 && (int)(now() - t) < -X_UC * 4; ++i) __builtin_amdgcn_s_sleep(X_UC);
#pragma unroll 1
    for (int i = 0; i < 4096 && (int)(now() - t) < 0; ++i) __builtin_amdgcn_s_sleep(1);
}
template <int PRE, int SLP = 2> DI int flags_ok(rsrc_t rws, u32 fo, u32 fs, u32 tag, u32x4& fl) {
    int spin = 0;
    if (PRE) vmw<0>();
#pragma unroll 1
    for (; spin < SPIN_CAP; ++spin) {
        PIN(fl);
        if (__builtin_amdgcn_ballot_w64(((fl.x ^ tag) | (fl.y ^ tag) | (fl.z ^ tag) | (fl.w ^ tag)) != 0) == 0) break;
        __builtin_amdgcn_s_sleep(SLP);
        gl1<0>(fl, rws, fo, fs);
        vmw<0>();
    }
    return spin;
}
template <int CTRL, int RM> DI u32 dmax(u32 v) { u32 o = __builtin_amdgcn_update_dpp(0, v, CTRL, RM, 0xf, true); return o > v ? o : v; }
template <int PRE, int SLP = 1> DI u32 hint_wait(rsrc_t rws, u32 ho, u32 hs, u32 tag, u32x4& hv, int& spin) {
    if (PRE) vmw<0>();
#pragma unroll 1
    for (spin = 0; spin < SPIN_CAP; ++spin) {
        PIN(hv);
        if (__builtin_amdgcn_ballot_w64(((hv.y ^ tag) | (hv.w ^ tag)) != 0) == 0) break;
        __builtin_amdgcn_s_sleep(SLP);
        gl1<0>(hv, rws, ho, hs);
        vmw<0>();
    }
    const u32 t0 = now() - 0x40000000u;
    u32 m = max(hv.x - t0, hv.z - t0);
    m = dmax<0x111, 0xf>(m); m = dmax<0x112, 0xf>(m); m = dmax<0x114, 0xf>(m); m = dmax<0x118, 0xf>(m);
    return t0 + __builtin_amdgcn_readlane(m, 15);
}
DI u32 max3(u32 a, u32 b, u32 c) { u32 m = a > b ? a : b; return m > c ? m : c; }
DI u32 mx4(u32x4 v, u32 m) { return max3(max3(m, v.x, v.y), v.z, v.w); }
DI u32 nos(u32 v) { return v < SENT - 1 ? v : SENT - 1; }
DI void gl4_if64(u64 cond, rsrc_t rs, u32 voff, u32 soff, u32x4& a, u32x4& b, u32x4& c, u32x4& d) {
    asm volatile("s_cmp_eq_u64 %7, 0\n\ts_cbranch_scc1 1f\n\t"
                 "buffer_load_dwordx4 %0, %4, %5, %6 offen sc1\n\tbuffer_load_dwordx4 %1, %4, %5, %6 offen offset:1024 sc1\n\t"
                 "buffer_load_dwordx4 %2, %4, %5, %6 offen offset:2048 sc1\n\tbuffer_load_dwordx4 %3, %4, %5, %6 offen offset:3072 sc1\n1:"
                 : "+v"(a), "+v"(b), "+v"(c), "+v"(d) : "v"(voff), "s"(rs), "s"(soff), "s"(cond) : "memory", "scc");
}
template <int N, int YOUNG, int TAG, int GS = 4096>
DI int swait(rsrc_t rs, u32 voff, u32 soff, u32 tag, u32x4 (&v)[N]) {
    vmw<YOUNG>();
    int spin = 0;
#pragma unroll 1
    for (; spin < SPIN_CAP; ++spin) {
        u64 bad[N / 4], any = 0;
#pragma unroll
        for (int k = 0; k < N; k += 4) {
            tie(v[k], v[k + 1], v[k + 2], v[k + 3]);
            if (TAG) {
                u32 m = 0;
#pragma unroll
                for (int q = 0; q < 4; ++q) m |= (v[k + q].y ^ tag) | (v[k + q].w ^ tag);
                bad[k / 4] = __builtin_amdgcn_ballot_w64(m != 0);
            } else {
                bad[k / 4] = __builtin_amdgcn_ballot_w64(mx4(v[k + 3], mx4(v[k + 2], mx4(v[k + 1], mx4(v[k], 0)))) == SENT);
            }
            any |= bad[k / 4];
        }
        if (any == 0) break;
#pragma unroll
        for (int k = 0; k < N; k += 4) gl4_if64(bad[k / 4], rs, voff, soff + (k / 4) * GS, v[k], v[k + 1], v[k + 2], v[k + 3]);
        vmw<0>();
    }
#pragma unroll
    for (int k = 0; k < N; k += 4) tie(v[k], v[k + 1], v[k + 2], v[k + 3]);
    return spin;
}
typedef _Float16 h2 __attribute__((ext_vector_type(2)));
typedef _Float16 f16x8 __attribute__((ext_vector_type(8)));
#define MFMA32H(a, b, c) __builtin_amdgcn_mfma_f32_32x32x16_f16(__builtin_bit_cast(f16x8, a), __builtin_bit_cast(f16x8, b), (c), 0, 0, 0)
DI h2 hm(h2 z, h2 m) { h2 t = z * m.xx, n = {-m.y, m.y}; return __builtin_elementwise_fma(z.yx, n, t); }
DI h2 hc(h2 z, h2 m) { h2 t = z * m.xx, n = {m.y, -m.y}; return __builtin_elementwise_fma(z.yx, n, t); }
DI h2 pkh(float a, float b) { return __builtin_convertvector((f32x2){a, b}, h2); }
DI void sq2(float& ar, float& ai, float& br, float& bi, h2& pa, h2& pb) {
    float t0, t1, t2, t3;
    asm("v_mul_f32 %6, %1, %1\n\tv_mul_f32 %7, %3, %3\n\tv_add_f32 %8, %0, %0\n\tv_add_f32 %9, %2, %2\n\t"
        "v_fma_f32 %0, %0, %0, -%6\n\tv_fma_f32 %2, %2, %2, -%7\n\tv_mul_f32 %1, %1, %8\n\tv_mul_f32 %3, %3, %9\n\t"
        "v_cvt_pk_f16_f32 %4, %0, %1\n\tv_cvt_pk_f16_f32 %5, %2, %3"
        : "+v"(ar), "+v"(ai), "+v"(br), "+v"(bi), "=&v"(pa), "=&v"(pb), "=&v"(t0), "=&v"(t1), "=&v"(t2), "=&v"(t3));
}
constexpr u32 B_TROW = 272, B_TW = 53 * 272, B_U = 4 * B_TW, B_CS = 8256, B_KO = 1216, B_K3 = 2432, B_K4 = 2944, B_X = 12288;
DI u32 hcl(u32 r, u32 d) { u32 o; asm("v_pk_add_f16 %0, %1, %2 neg_lo:[0,1] neg_hi:[0,1] clamp" : "=v"(o) : "s"(r), "v"(d)); return o; }
template <int R0, int R1, int R2, int R3>
DI void put4(h2 a, h2 b, h2 c, h2 d) {
    asm volatile("ds_write_addtid_b32 %0 offset:%4\n\tds_write_addtid_b32 %1 offset:%5\n\tds_write_addtid_b32 %2 offset:%6\n\tds_write_addtid_b32 %3 offset:%7"
                 :: "v"(a), "v"(b), "v"(c), "v"(d), "n"(R0 * B_TROW), "n"(R1 * B_TROW), "n"(R2 * B_TROW), "n"(R3 * B_TROW) : "memory");
}
template <int R0, int R1>
DI void put2(h2 a, h2 b) {
    asm volatile("ds_write_addtid_b32 %0 offset:%2\n\tds_write_addtid_b32 %1 offset:%3" :: "v"(a), "v"(b), "n"(R0 * B_TROW), "n"(R1 * B_TROW) : "memory");
}
#define B_PF 0
#define B_SLP 2
#define PRIO(n) __builtin_amdgcn_s_setprio(n)
#define PINF(x) asm("" : "+v"(x))

DI void role_b(int bb, const u16* lar, const u16* aim, const u16* cre, const u16* cim,
               const u16* ldt, const u16* dsk, u16* out, char* ws, u32 pbv = 0) {
    const int tid = threadIdx.x, lane = tid & 63;
    const int w = __builtin_amdgcn_readfirstlane(tid >> 6);
    const int cg = bb % 7, bi = (bb / 7) & 31;
    const int ch = w >> 1, hw = w & 1;
    const int c = cg * 64 + bi * 2 + ch;
    const int l15 = lane & 15, g = lane >> 4;
    PRIO(3);
    asm volatile("" : "+s"(ws), "+s"(lar));
    rsrc_t rws = mk_rsrc(ws, WS_USED);
    u32 tz = now();
    const u32 tb = w * B_TW;
    if (bb >= 224) {
        const u32 t0 = now(), tg = mk_tag(sld(ws + WS_EPOCH + 32)), v = (bb - 224) * 4 + w, vo = lane * 16;
        st128(rws, lane < 16 ? WS_KVG + v * 256 + vo : OOB, (u32x4){SENT, SENT, SENT, SENT}, 1);
        vmw<0>();
        asm volatile("" :: "v"(pbv));
        BAR();
        if (w == 0) __builtin_amdgcn_raw_buffer_store_b32(tg, rws, lane ? OOB : 0, WS_CFLAG + (bb - 224) * 4, 16);
        return;
        const u32 one = l15 & 1 ? 0x3f800000u : 0x00003f80u;
        u32x4 s0 = {l15 < 2 ? one : 0u, 0u, (l15 >> 1) == 1 ? one : 0u, 0u}, s1 = {(l15 >> 1) == 2 ? one : 0u, 0u, (l15 >> 1) == 3 ? one : 0u, 0u};
        u32 ko = lane < 32 ? WS_KVG + v * 256 + g * 128 + (lane & 7) * 16 + (lane & 8) : OOB;
        u32 go = lane < 8 ? WS_H2 + ((v & 31) * 8 + lane) * 128 + (v >> 5) * 8 : OOB;
        PIN(ko); PIN(s0); PIN(s1); PIN(go);
        const u32 hs = WS_H1 + (bb - 224) * 128, ps = WS_PART + v * 2 * PT_S;
        u32x4 hv, pv[8];
        asm volatile("" : "=v"(hv));
#pragma unroll
        for (int k = 0; k < 8; ++k) asm volatile("" : "=v"(pv[k]));
        until(t0 + 300);
        gl1<0>(hv, rws, vo & 112, hs);
        int sp;
        const u32 t1 = hint_wait<1>(rws, vo & 112, hs, tg, hv, sp);
        __builtin_amdgcn_raw_buffer_store_b64((u32x2){t1, tg}, rws, go, 0, 16);
        until(t1 + A_D1);
        gload<8>(rws, vo, ps, PT_S, pv);
        sp = swait<8, 0, 1, PT_S>(rws, vo, ps, tg, pv);
        f32x4 a = {0.f, 0.f, 0.f, 0.f}, b = a;
#pragma unroll
        for (int k = 0; k < 4; ++k) { a = MFMA16(s0, pv[k], a); b = MFMA16(s1, pv[4 + k], b); }
        a += b;
        __builtin_amdgcn_raw_buffer_store_b64((u32x2){nos(pk2(a[0], a[1])), nos(pk2(a[2], a[3]))}, rws, ko, 0, 16);
        return;
    }
    u32 cpf = min(tid * 128u, B_PF - 128u);
    u32 po = lane * 4, ps = c * 512 + hw * 256;
    u32 p_lar = __builtin_amdgcn_raw_buffer_load_b32(mk_rsrc(lar, 448 * 512), po, ps, 0);
    u32 p_aim = __builtin_amdgcn_raw_buffer_load_b32(mk_rsrc(aim, 448 * 512), po, ps, 0);
    u32 p_cre = __builtin_amdgcn_raw_buffer_load_b32(mk_rsrc(cre, 448 * 512), po, ps, 0);
    u32 p_cim = __builtin_amdgcn_raw_buffer_load_b32(mk_rsrc(cim, 448 * 512), po, ps, 0);
    u32 ldt2 = sld(ldt + (c & ~1)), dsk2 = sld(dsk + (c & ~1)), ep = sld(ws + WS_EPOCH + cg * 4);
    asm volatile("" : "+s"(ldt2), "+s"(dsk2), "+s"(ep));
    u32 tag = mk_tag(ep);
    SB();
    asm volatile("s_mov_b32 m0, %0" :: "s"(tb) : "memory");
    const float dt = rbf(expf(blo(ldt2 >> (16 * ch))));
    float ke = -dt * 1.44269504088896f, kr = dt * 0.159154943091895f;
    float dskip = __uint_as_float(tid & 127 ? 0u : (dsk2 >> (16 * ch)) << 16);
    const int sl = lane & 31;
    const u32 hrow = sl - (u32)(((sl < 16 ? 0x3376111111100000ull : 0xcccc7bbb37770000ull) >> (4 * (sl & 15))) & 15);
    lptr trl = lp(tb + sl * B_TROW + (lane >> 5) * 16);
    lptr trh = lp(tb + (32 + hrow) * B_TROW + (lane >> 5) * 16);
    const u32 cb = B_U + ch * B_CS;
    { const lptr z = lp(cb + (tid & 127) * 16); st<u32x4>(z, 0, (u32x4){0u, 0u, 0u, 0u}); st<u32x4>(z, 2048, (u32x4){0u, 0u, 0u, 0u}); }
    u32x4 b3, b5, bx, by;
    {
        const float e0 = (float)(12 + l15 - 8 * g);
        h2 e = pkh(e0, e0 - 1.f);
        u32 sg[6], ab[6];
#pragma unroll
        for (int q = 0; q < 6; ++q) { sg[q] = __builtin_bit_cast(u32, e); ab[q] = sg[q] & 0x7fff7fffu; e -= (h2){2, 2}; }
#pragma unroll
        for (int q = 0; q < 4; ++q) {
            b3[q] = hcl(0x40004000u, ab[q]); b5[q] = hcl(0x42004200u, ab[q + 2]); by[q] = hcl(0x45004500u, ab[q + 2]);
            bx[q] = hw ? hcl(0x44004400u, ab[q + 2]) : b3[q] & hcl(0x3c003c00u, sg[q]);
        }
        asm volatile("" : "+v"(b3), "+v"(b5), "+v"(bx), "+v"(by));
    }
    asm volatile("ds_write_addtid_b32 %0" :: "v"(0x00003c00u) : "memory");
    u32 vo = lane * 16;
    asm volatile("" : "+v"(ke), "+v"(kr), "+v"(dskip), "+v"(vo), "+v"(trl), "+v"(trh));
    asm volatile("" : "+v"(p_lar), "+v"(p_aim), "+v"(p_cre), "+v"(p_cim) : "v"(cpf), "v"(pbv) : "memory");
    PRIO(0);
    int sh;
    {
        u32 a = p_cre & 0x7f807f80u, b = p_cim & 0x7f807f80u;
        u32 m = max(max(a, b), max(a << 16, b << 16));
        m = dmax<0x111, 0xf>(m); m = dmax<0x112, 0xf>(m); m = dmax<0x114, 0xf>(m); m = dmax<0x118, 0xf>(m);
        m = dmax<0x142, 0xa>(m); m = dmax<0x143, 0xc>(m);
        sh = 140 - (int)(__builtin_amdgcn_readlane(m, 63) >> 23);
    }
    f32x16 kd;
    u32x4 gv[4], lo[8], hi[8], ud[2], uf;
    asm volatile("" : "=v"(uf));
    bool late;
    asm volatile("" : "=v"(ud[0]), "=v"(ud[1]));
#define ULD(o) asm volatile("buffer_load_dwordx4 %0, %2, %3, %4 offen sc1\n\tbuffer_load_dwordx4 %1, %2, %3, %4 offen offset:1024 sc1" : "+v"(ud[0]), "+v"(ud[1]) : "v"(o), "s"(rws), "s"(gs) : "memory");
    u32 uo = (vo & 48) | OOB;
#pragma unroll
    for (int k = 0; k < 8; ++k) asm volatile("" : "=v"(lo[k]), "=v"(hi[k]));
    u32 gs = WS_UT + (cg * 32 + bi) * UT_S + w * 2048, fs = WS_UF + (cg * 4 + w) * 128;
    lptr uw = lp(B_U + ((lane >> 2) & 1) * B_CS + 4096 + w * 1024 + (lane >> 3) * 64 + (lane & 3) * 16);
    lptr gp = lp(cb + 4096 - 32 - 16 * hw + 32 * l15 + 16 * g);
    lptr xr = lp((w ^ 1) * B_TW + lane * 16);
    asm volatile("" : "+s"(gs), "+s"(fs), "+v"(tag), "+v"(uw), "+v"(gp), "+v"(xr));
#pragma unroll
    for (int i = 0; i < 16; ++i) kd[i] = 0.f;
#pragma unroll 1
    for (int rnd = 0; rnd < 2; ++rnd) {
        if (rnd == X_BR && (X_BA || blockIdx.x < 448)) { for (int i = 0; i < 512 && (int)(now() - tz - X_BP) < 0; ++i) __builtin_amdgcn_s_sleep(4); }
        gl1<0>(uf, rws, uo, fs);
        uo &= 48;
        SB();
#define KM(s) { kd = MFMA32H(hi[s], lo[s], kd); asm volatile("" : "+v"(kd)); }
#define KMI(s) { int r_ = rnd; asm volatile("" : "+s"(r_)); if (r_) KM(s) }
        KMI(0)
        float e2 = rbf(__builtin_amdgcn_exp2f(blo(p_lar) * 1.44269504088896f)) * ke;
        float rev = blo(p_aim) * kr;
        float fa = __builtin_amdgcn_fractf(rev), fb = __builtin_amdgcn_fractf(rev * 32.f);
        float ma = __builtin_amdgcn_exp2f(e2), mb = __builtin_amdgcn_exp2f(e2 * 32.f);
        KMI(1)
        float ar = __builtin_amdgcn_cosf(fa), ai = __builtin_amdgcn_sinf(fa), br = __builtin_amdgcn_cosf(fb), bi_ = __builtin_amdgcn_sinf(fb);
        KMI(2)
        ar *= ma; PINF(ar); ai *= ma; PINF(ai); br *= mb; PINF(br); bi_ *= mb; PINF(bi_);
        const h2 C = pkh(__builtin_ldexpf(blo(p_cre), sh), -__builtin_ldexpf(blo(p_cim), sh));
        p_lar >>= 16; p_aim >>= 16; p_cre >>= 16; p_cim >>= 16;
        h2 w1 = pkh(ar, ai), w32 = pkh(br, bi_), w2, w4, w8, w16, w64, w128, w256, w512, w1k, wx;
        KMI(3)
        sq2(ar, ai, br, bi_, w2, w64);
        KMI(4)
        sq2(ar, ai, br, bi_, w4, w128);
        KMI(5)
        sq2(ar, ai, br, bi_, w8, w256);
        KMI(6)
        sq2(ar, ai, br, bi_, w16, w512);
        KMI(7)
        { float t = bi_ * bi_; PINF(t); float d = br + br; PINF(d); t = __builtin_fmaf(br, br, -t); PINF(t); w1k = pkh(t, d * bi_); }
        asm volatile("s_waitcnt vmcnt(0)" : "+v"(uf), "+v"(w1k) :: "memory");
        late = __builtin_amdgcn_ballot_w64(((uf.x ^ tag) | (uf.y ^ tag) | (uf.z ^ tag) | (uf.w ^ tag)) != 0) != 0;
        ULD(late ? OOB : vo)
        h2 L[16], H1, H2, H3, H4, G1, G2, G3, C8, a, b;
        L[1] = w8; L[2] = w16; L[4] = w32; L[8] = w64;
        put4<1, 2, 4, 8>(w8, w16, w32, w64);
        L[3] = hm(w16, w8); H1 = hc(C, w1); H2 = hc(C, w2); H4 = hc(C, w4);
        put4<16, 32, 3, 33>(w128, C, L[3], H1);
        L[5] = hm(L[1], w32); L[6] = hm(L[2], w32); L[7] = hm(L[3], w32); H3 = hc(H2, w1);
        put4<34, 39, 5, 6>(H2, H4, L[5], L[6]);
        a = hc(H1, w4); b = hc(H2, w4); wx = hc(H3, w4); G1 = hc(C, w256);
        put4<7, 35, 40, 41>(L[7], H3, a, b);
        L[9] = hm(L[1], w64); L[10] = hm(L[2], w64); L[11] = hm(L[3], w64); L[12] = hm(L[4], w64);
        put4<42, 36, 9, 10>(wx, G1, L[9], L[10]);
        L[13] = hm(L[5], w64); L[14] = hm(L[6], w64); L[15] = hm(L[7], w64); a = hc(G1, w2);
        put4<11, 12, 13, 14>(L[11], L[12], L[13], L[14]);
        b = hc(G1, w4); wx = hc(a, w4); H1 = hc(G1, w8); C8 = hc(C, w8);
        put4<15, 37, 38, 43>(L[15], a, b, wx);
        H4 = hc(H4, w512); G2 = hc(C8, w512); b = hc(b, w512); G3 = hc(H1, w512);
        put4<44, 48, 49, 45>(H1, H4, G2, b);
        C8 = hc(C8, w1k); H1 = hc(H1, w1k); G2 = hc(G2, w1k); a = hc(G3, w1k);
        put4<46, 50, 51, 47>(G3, C8, H1, G2);
        L[1] = hm(L[1], w128); L[2] = hm(L[2], w128); L[3] = hm(L[3], w128); L[4] = hm(L[4], w128);
        put4<52, 17, 18, 19>(a, L[1], L[2], L[3]);
        L[5] = hm(L[5], w128); L[6] = hm(L[6], w128); L[7] = hm(L[7], w128); L[8] = hm(L[8], w128);
        put4<20, 21, 22, 23>(L[4], L[5], L[6], L[7]);
        L[9] = hm(L[9], w128); L[10] = hm(L[10], w128); L[11] = hm(L[11], w128); L[12] = hm(L[12], w128);
        put4<24, 25, 26, 27>(L[8], L[9], L[10], L[11]);
        L[13] = hm(L[13], w128); L[14] = hm(L[14], w128); L[15] = hm(L[15], w128);
        put4<28, 29, 30, 31>(L[12], L[13], L[14], L[15]);
#pragma unroll
        for (int s = 0; s < 8; ++s) {
            hi[s] = ld<u32x4>(trh, s * 32); lo[s] = ld<u32x4>(trl, s * 32);
        }
        SB();
    }
    if (__builtin_expect(late, 0)) {
        gl1<0>(uf, rws, uo, fs);
        int sp = flags_ok<1, B_SLP>(rws, uo, fs, tag, uf);
        ULD(vo)
    }
    SB();
    asm volatile("s_waitcnt vmcnt(0)" : "+v"(ud[0]), "+v"(ud[1]) :: "memory");
    st<u32x4>(uw, 0, ud[0]); st<u32x4>(uw, 512, ud[1]);
    SB();
    KM(0) KM(1) KM(2) KM(3) KM(4) KM(5) KM(6) KM(7)
#undef KM
    lptr fp = gp + (-240 * hw - 192);
    lptr kp = lp(cb + (~l15 & 1) * B_KO + (((527 - l15) >> 1) + 4 * g - 16 * 11 - 64 * hw) * 4);
    lptr k3p = lp(cb + B_K3 + 256 * hw + (15 - l15 + 4 * g) * 4);
    PIN(fp); PIN(kp); PIN(k3p);
    const float ks = __builtin_ldexpf(dt, 1 - sh), kt = ks * 128.f;
    const u32 hs = WS_H3 + (cg * 4 + (bi & 3)) * 256;
    const f32x4 z4 = {0.f, 0.f, 0.f, 0.f};
    u32x4 G[6], F[2][8], kn[2][2], k3[3], wa[6];
    f32x4 acc[8];
#define LD4(d, p, o) _Pragma("unroll") for (int q_ = 0; q_ < 4; ++q_) d[q_] = ld<u32>(p, (o) + q_ * 4);
#define LDF(r) _Pragma("unroll") for (int i_ = 0; i_ < 8; ++i_) F[(r) & 1][i_] = ld<u32x4>(fp, i_ * 512 + (3 - (r)) * 64);
#define LDK(r) { LD4(kn[(r) & 1][0], kp, (11 - (r)) * 64) LD4(kn[(r) & 1][1], kp, (3 - (r)) * 64) }
#define LDG() _Pragma("unroll") for (int i_ = 0; i_ < 6; ++i_) G[i_] = ld<u32x4>(gp, i_ * 512);
#define LDK3() _Pragma("unroll") for (int q2 = 0; q2 < 3; ++q2) LD4(k3[q2], k3p, (2 - q2) * 64)
#define PK4(d, x, y) { d = (u32x4){pk2(x[0], y[0]), pk2(x[1], y[1]), pk2(x[2], y[2]), pk2(x[3], y[3])}; PIN(d); }
#define WIN(i0, i1) _Pragma("unroll") for (int i_ = i0; i_ < i1; ++i_) { f32x4 x_ = MFMA16(G[i_], b3, z4), y_ = MFMA16(G[i_], b5, z4); PK4(wa[i_], x_, y_) }
#define B_W0 2
#define B_W1 2
    const lptr xw = lp(tb + lane * 16);
    if (hw == 0) {
        f32x4 p0, p1;
#pragma unroll
        for (int i = 0; i < 4; ++i) { p0[i] = kd[8 + i] * kt; PIN(p0[i]); }
        st<f32x4>(xw, 0, p0);
        lptr ke = lp(cb + 1072 - 16 * lane), kz = lp(lane == 63 ? cb : tb + 2048 + lane * 8);
        PIN(ke); PIN(kz);
        bar();
        p0 = ld<f32x4>(xr, 0); p1 = ld<f32x4>(xr, 1024);
        LDG()
        SB();
#pragma unroll
        for (int i = 0; i < 4; ++i) { p1[3 - i] = __builtin_fmaf(kd[7 - i], ks, p1[3 - i]); PINF(p1[3 - i]); }
#pragma unroll
        for (int i = 0; i < 4; ++i) { p0[i] = __builtin_fmaf(kd[i], ks, p0[i]); PINF(p0[i]); }
        p0[0] += dskip;
        const u32x4 e = {pk2(p1[3], p1[2]), pk2(p1[1], p1[0]), pk2(p0[3], p0[2]), pk2(p0[1], p0[0])};
        st<u32x4>(ke, 0, e);
        const u32 pv = __builtin_amdgcn_update_dpp(0, e.x, 0x138, 0xf, 0xf, true), kk = PLO(e.x, e.x);
        st<u32x4>(ke, B_KO, (u32x4){__builtin_amdgcn_alignbit(e.y, e.x, 16), __builtin_amdgcn_alignbit(e.z, e.y, 16), __builtin_amdgcn_alignbit(e.w, e.z, 16), __builtin_amdgcn_alignbit(pv, e.w, 16)});
        st<u32x2>(kz, 56, (u32x2){e.x << 16, kk});
        st<u32x2>(kz, B_KO + 56, (u32x2){kk, kk});
        LDF(0)
        SB();
#pragma unroll
        for (int i = 0; i < 4; ++i) { acc[4 + i] = MFMA16(bx, G[i], z4); PIN(acc[4 + i]); }
        WIN(0, B_W0)
        SB();
        bar();
        u32x4 kx;
        const lptr kq = lp((u32)(size_t)kp - 320);
        LD4(kx, kq, 0)
        const float k1k = bhi(ld<u32>(lp(cb + B_K3 + 256 + 64), 0));
        LDK3() LDK(0)
        SB();
        if (ch == 0) __builtin_amdgcn_raw_buffer_store_b64((u32x2){now(), tag}, rws, lane < 4 ? WS_H3 + (cg * 4 + lane) * 256 + bi * 8 : OOB, 0, 16);
        WIN(B_W0, 6)
        SB();
        acc[0] = acc[1] = z4;
        acc[2] = MFMA16(kx, G[0], z4);
        acc[4] *= k1k; acc[5] *= k1k;
        acc[3] = MFMA16(kx, G[1], z4);
        acc[6] *= k1k; acc[7] *= k1k;
#pragma unroll
        for (int i = 2; i < 6; ++i) acc[i + 2] = MFMA16(kx, G[i], acc[i + 2]);
    } else {
        f32x4 p0, p1;
#pragma unroll
        for (int i = 0; i < 4; ++i) { p0[i] = kd[i] * ks; PIN(p0[i]); p1[i] = kd[4 + i] * ks; PIN(p1[i]); }
        st<f32x4>(xw, 0, p0);
        st<f32x4>(xw, 1024, p1);
        lptr k3w = lp(cb + B_K3 + (lane & 1) * 256 + (47 - (lane >> 1)) * 4), k4w = lp(cb + B_K4 + (47 - (lane >> 5) * 32 - (sl >> 1)) * 4 + (lane & 1) * 2);
        PIN(k3w); PIN(k4w);
        bar();
        u32x4 A[4], wb[4], k4[4];
        p0 = ld<f32x4>(xr, 0);
#pragma unroll
        for (int i = 0; i < 4; ++i) A[i] = ld<u32x4>(gp, i * 512 + 16);
        LDG()
        SB();
#pragma unroll
        for (int i = 0; i < 4; ++i) { p0[i] = __builtin_fmaf(kd[8 + i], kt, p0[i]); PINF(p0[i]); }
        st<u32>(k3w, 0, pk2(p0[0], p0[1]));
        const u32 f = pk2(p0[2], p0[3]);
        st<u16>(k4w, 64, (u16)f);
        st<u16>(k4w, 0, (u16)(f >> 16));
        LDF(0)
        SB();
#pragma unroll
        for (int i = 0; i < 4; ++i) { f32x4 x = MFMA16(A[i], bx, z4), y = MFMA16(G[i], by, z4); PK4(wb[i], x, y) }
        WIN(0, B_W1)
        SB();
        bar();
        const lptr k4p = k3p + (B_K4 - B_K3 - 256);
#pragma unroll
        for (int q = 0; q < 4; ++q) LD4(k4[q], k4p, (3 - q) * 64)
        LDK3() LDK(0)
        SB();
        WIN(B_W1, 6)
        SB();
        acc[0] = acc[1] = acc[2] = acc[3] = z4;
#pragma unroll
        for (int i = 0; i < 4; ++i) acc[4 + i] = MFMA16(wb[i], k4[0], z4);
#pragma unroll
        for (int q = 1; q < 4; ++q)
#pragma unroll
            for (int i = 0; i + q < 4; ++i) acc[4 + i + q] = MFMA16(wb[i], k4[q], acc[4 + i + q]);
    }
    SB();
#pragma unroll
    for (int q = 0; q < 3; ++q)
#pragma unroll
        for (int i = 0; i + q < 6; ++i) acc[2 + i + q] = MFMA16(wa[i], k3[q], acc[2 + i + q]);
    SB();
#pragma unroll
    for (int r = 0; r < 4; ++r) {
        const lptr yw = lp(B_X + w * 8192 + lane * 16);
        if (r < 3) { LDK(r + 1) LDF(r + 1) }
#pragma unroll
        for (int i = 0; i < 8; ++i) {
            acc[i] = MFMA16(kn[r & 1][0], F[r & 1][i], acc[i]);
            if (i < 7) acc[i + 1] = MFMA16(kn[r & 1][1], F[r & 1][i], acc[i + 1]);
        }
        if (r < 3) {
#pragma unroll
            for (int n = 0; n < 12; ++n) { __builtin_amdgcn_sched_group_barrier(0x008, 1, 0); __builtin_amdgcn_sched_group_barrier(0x100, 1, 0); }
        } else {
#pragma unroll
            for (int k = 0; k < 8; ++k) st<f32x4>(yw, k * 1024, acc[k]);
            __builtin_amdgcn_sched_group_barrier(0x008, 2, 0);
#pragma unroll
            for (int n = 0; n < 7; ++n) { __builtin_amdgcn_sched_group_barrier(0x008, 2, 0); __builtin_amdgcn_sched_group_barrier(0x200, 1, 0); }
        }
        SB();
    }
#undef LD4
#undef LDF
#undef LDK
#undef LDG
#undef LDK3
#undef PK4
#undef WIN
    u32x4 hv;
    asm volatile("buffer_load_dwordx4 %0, %1, %2, %3 offen sc1" : "=&v"(hv) : "v"(w ? OOB : vo & 240), "s"(rws), "s"(hs) : "memory");
    {
        const lptr xr = lp(B_X + w * 2048 + lane * 16);
        const u32 so = WS_YT + (u32)(cg * 128 + w * 32 + (l15 >> 1) * 2) * YT_S + bi * 128 + ((l15 & 1) * 4 + g) * 16;
        bar();
        f32x4 o[8];
#pragma unroll
        for (int k = 0; k < 8; ++k) o[k] = ld<f32x4>(xr, (k & 3) * 8192 + (k >> 2) * 1024);
        SB();
#pragma unroll
        for (int e = 0; e < 2; ++e) {
            u32 d[4];
#pragma unroll
            for (int i = 0; i < 4; ++i) { float a = o[4 * e][i] + o[4 * e + 1][i], b = o[4 * e + 2][i] + o[4 * e + 3][i]; PINF(a); PINF(b); d[i] = pk2(a, b); }
            st128(rws, so + e * 16 * YT_S, (u32x4){d[0], tag, d[1], tag}, 1);
            st128(rws, so + (e * 16 + 1) * YT_S, (u32x4){d[2], tag, d[3], tag}, 1);
        }
    }
    {
        const u32 ys = WS_YT + (cg * 128 + bi * 4 + w) * YT_S, yo = (lane >> 3) * 512 + (lane & 7) * 16;
#define YLD() asm volatile("buffer_load_dwordx4 %0, %4, %5, %6 offen sc1\n\tbuffer_load_dwordx4 %1, %4, %5, %6 offen offset:128 sc1\n\t" \
                 "buffer_load_dwordx4 %2, %4, %5, %6 offen offset:256 sc1\n\tbuffer_load_dwordx4 %3, %4, %5, %6 offen offset:384 sc1\n\ts_waitcnt vmcnt(0)" \
                 : "=&v"(gv[0]), "=&v"(gv[1]), "=&v"(gv[2]), "=&v"(gv[3]) : "v"(yo), "s"(rws), "s"(ys) : "memory")
        const u32 sw = ((lane >> 3) ^ lane) & 7;
        const lptr sa = lp(B_U + w * 2048 + (lane & 7) * 128 + sw * 16), ra = lp(B_U + w * 2048 + (lane >> 3) * 128 + sw * 16);
        const u32 oo = (u32)((bi >> 2) * 256 + ((bi & 3) * 2 + (w >> 1)) * 32 + (w & 1) * 2 + (lane >> 3) * 4) * 1792 + 896 + cg * 128 + (lane & 7) * 16;
        int spin = 0;
        asm volatile("s_waitcnt vmcnt(4)" : "+v"(hv) :: "memory");
        if (w == 0) {
            int sp;
            until(hint_wait<0, 4>(rws, vo & 240, hs, tag, hv, sp) + B_D3);
        }
        BAR();
#pragma unroll 1
        for (; spin < SPIN_CAP; ++spin) {
            YLD();
            u32 bad = 0;
#pragma unroll
            for (int k = 0; k < 4; ++k) bad |= (gv[k].y ^ tag) | (gv[k].w ^ tag);
            if (__builtin_amdgcn_ballot_w64(bad != 0) == 0) break;
        }
#undef YLD
        st<u32x4>(sa, 0, (u32x4){gv[0].x, gv[1].x, gv[2].x, gv[3].x});
        st<u32x4>(sa, 1024, (u32x4){gv[0].z, gv[1].z, gv[2].z, gv[3].z});
        cbar();
        u32x4 r0 = ld<u32x4>(ra, 0), r1 = ld<u32x4>(ra, 1024);
        rsrc_t ro = mk_rsrc(out, 2048 * 896 * 2);
        st128(ro, oo, r0, 1);
        st128(ro, oo + 1792, r1, 1);
        if (bi == 0 && tid == 0) __builtin_amdgcn_raw_buffer_store_b32(tag, rws, 0, WS_EPOCH + cg * 4, 16);
    }
}
constexpr u32 A_W = 49152;
constexpr int A_NA = 5, A_NX = 8;
constexpr int Q_NA = 5, Q_NX = 8;
#define NH1 4
#define HPOS 0
#define X_NOPQ 0
#define X_NOKV 0
#define H2V (vo & 16)
#define H2S (WS_H2 + b * 128)
constexpr float A_ONE = 1.000001f;
template <int V> DI void phi_step(const f32x16 (&a)[2], float c2, float (&e)[2], float& m, u32x4 (&pf)[2][2]) {
    constexpr int U = V - 2;
    float y;
    if constexpr (U < 0) {
        asm volatile("v_fmamk_f32 %0, %2, 0x3fb8aa3b, %3\n\tv_exp_f32 %1, %0 clamp" : "=&v"(y), "=v"(e[V & 1]) : "v"(a[V >> 4][V & 15]), "v"(c2));
    } else if constexpr (V < 32) {
        if constexpr (V & 1) {
            u32 r;
            asm volatile("v_add_f32 %1, 0x358637bd, %1\n\tv_fmamk_f32 %0, %3, 0x3fb8aa3b, %4\n\tv_max_f32 %1, %1, %5\n\tv_cvt_pk_bf16_f32 %2, %6, %1\n\tv_exp_f32 %1, %0 clamp"
                         : "=&v"(y), "+v"(e[1]), "=&v"(r) : "v"(a[V >> 4][V & 15]), "v"(c2), "v"(a[U >> 4][U & 15]), "v"(m));
            pf[U >> 4][(U >> 3) & 1][(U >> 1) & 3] = r;
        } else {
            asm volatile("v_add_f32 %1, 0x358637bd, %1\n\tv_fmamk_f32 %0, %3, 0x3fb8aa3b, %4\n\tv_max_f32 %2, %1, %5\n\tv_exp_f32 %1, %0 clamp"
                         : "=&v"(y), "+v"(e[0]), "=&v"(m) : "v"(a[V >> 4][V & 15]), "v"(c2), "v"(a[U >> 4][U & 15]));
        }
    } else if constexpr (V == 32) {
        asm volatile("v_add_f32 %0, 0x358637bd, %0\n\tv_max_f32 %1, %0, %2" : "+v"(e[0]), "=v"(m) : "v"(a[1][14]));
    } else {
        u32 r;
        asm volatile("v_add_f32 %0, 0x358637bd, %0\n\tv_max_f32 %0, %0, %2\n\tv_cvt_pk_bf16_f32 %1, %3, %0" : "+v"(e[1]), "=v"(r) : "v"(a[1][15]), "v"(m));
        pf[1][1][3] = r;
    }
}
template <int V0, int V1> DI void phi_run(const f32x16 (&a)[2], float c2, float (&e)[2], float& m, u32x4 (&pf)[2][2]) {
    if constexpr (V0 < V1) { phi_step<V0>(a, c2, e, m, pf); phi_run<V0 + 1, V1>(a, c2, e, m, pf); }
}
DI u32x4 ldw(lptr p, int off) { u32x2 a = ldtr(p, off), b = ldtr(p, off + 1024); return (u32x4){a.x, a.y, b.x, b.y}; }
DI u32x4 ldw2(lptr p, int off) { u32x2 a = ldtr(p, off), b = ldtr(p, off + 512); return (u32x4){a.x, a.y, b.x, b.y}; }
#define BARN(n) asm volatile("s_waitcnt lgkmcnt(" #n ")\n\ts_barrier" ::: "memory")
DI f32x16 splat16(float v) { f32x16 r; _Pragma("unroll") for (int i = 0; i < 16; ++i) r[i] = v; return r; }


DI void role_a(int b, u64 pc, u32 epf, const u16* x, const u16* wqkv, const u16* wo, const u16* u, u16* out, char* ws) {
    const int tid = threadIdx.x, lane = tid & 63;
    const int w = __builtin_amdgcn_readfirstlane(tid >> 6);
    const int l15 = lane & 15, g = lane >> 4, n = lane & 31, hh = lane >> 5;
#define jj (b >> 6)
#define idx (b >> 3)
#define t (b >> 5)
#define R (((b >> 1) & 3) * 4 + (idx & 3))
#define rh (w >> 1)
#define hd (w & 1)
    u32 ep, so[7], xs[7], va[2], xo[4], xq, sb, xr;
    const u32 xb = w * 4096, wb = A_W + w * 2048;
    rsrc_t rws, ro;
#define rg (((b >> 1) & 3) * 8 + (idx & 7))
#define r32 (2 * rg + (b & 1))
#define cj (jj ^ (jj >> 1))
#define act (cj < 3 || w < 2)
#define wso (cj * 256 + w * 64)
    const u32 vo = lane * 16;
    u32 wv[7], wq[4], tag, wl;
#define WQ() { u32 w_ = w; asm volatile("" : "+s"(w_)); _Pragma("unroll") for (int r = 0; r < 4; ++r) wq[r] = cj * 256 + w_ * (112 * 896) + (8 * (r & 1) + (r >> 1)) * 896 + 4096 - r * 1024; }
#define LATE0() { asm volatile("" : "+s"(ws), "+s"(wo)); ep = sld(ws + WS_EPOCH + 32); \
        rws = mk_rsrc(ws, WS_USED); ro = mk_rsrc((const char*)wo - 4096, 448 * 896 + 4096); WQ() }
#define SLAB(c) { asm volatile("" : "+s"(xq)); xq = min(xq + 128, xq - 768); so[c] = xq * 1344 + sb; xs[c] = xq + xr; \
        if (c >= A_NA || (c >= Q_NA && jj == 2)) { u32 w_ = w; asm volatile("" : "+s"(w_)); so[c] += (w_ >> 1) * 43008u + (w_ & 1) * (jj < 2 ? 42112u : 42896u); } }
    lptr wp, xp[4], wpa[4];
    u32x4 wf[3], xf[A_NX > Q_NX ? A_NX : Q_NX][2], pf[2][2], wr[28];
    f32x16 acc[2][2];
    float pc2, pe[2], pm;
    u32x4 pq[8], kv[8], fl, fq, hv;
#define SETUP() { SB(); const u32 cq = (g & 1) * 4 + (l15 & 3); \
        wp = lp(A_W + (8 * hh + (l15 >> 2)) * 128 + (((cq < 7 ? cq + 7 * hd : 14 + (hd ^ 1)) * 8) ^ ((l15 & 8) * 8))); PIN(wp); \
        _Pragma("unroll") for (int q = 0; q < 4; ++q) { xp[q] = lp(rh * 8192 + n * 128 + (((2 * q + hh) ^ (n >> 1)) & 7) * 16); PIN(xp[q]); } \
        acc[0][0] = splat16(A_ONE); PIN(acc[0][0]); acc[0][1] = acc[0][0]; SB(); }
#define AUX0() { pc2 = -1.44269504088896f * A_ONE; PIN(pc2); }
#define AUX1() { const u32 ct = (l15 >> 2) ^ g; wv[0] = g * 1792 + (ct & 1) * 64 + (ct >> 1) * (cj < 3 ? 128 : 14336) + (lane & 3) * 16; PIN(wv[0]); }
#define AUX2() { const u32 fs_ = cj < 3 ? 14336 : 28672; _Pragma("unroll") for (int f = 1; f < 7; ++f) { wv[f] = wv[f - 1] + fs_; PIN(wv[f]); } }
#define AUX3() { wl = (l15 >> 2) * 256 + ((l15 >> 2) ^ w) * 64 + ((l15 & 1) * 4 + (g & 1) * 2 + ((l15 >> 1) & 1)) * 8; PIN(wl); \
        _Pragma("unroll") for (int r = 0; r < 4; ++r) { wpa[r] = lp(wl + hh * 2048 + ((w + r) & 3) * 4096); PIN(wpa[r]); } }
#define KEEP() asm volatile("" :: "v"(epf));
#define TAG() { asm volatile("" : "+s"(ep)); tag = mk_tag(ep); }
#define WSL()
#define WO4(f, slot) { M0(xb, (slot) * 16384); NOP(); \
        dmao<0>(ro, wv[f], wq[0]); WSL() dmao<1024>(ro, wv[f], wq[1]); WSL() dmao<2048>(ro, wv[f], wq[2]); WSL() dmao<3072>(ro, wv[f], wq[3]); WSL() }
#define WO2(f, s0, s1) { M0(wb, (s0) * 8192); NOP(); dmao<0>(ro, wv[f], wq[0]); WSL() dmao<1024>(ro, wv[f], wq[1]); WSL() \
        M0(wb, (s1) * 8192 - 2048); NOP(); dmao<2048>(ro, wv[f], wq[2]); WSL() dmao<3072>(ro, wv[f], wq[3]); WSL() }
#define WRD(f, slot) _Pragma("unroll") for (int r = 0; r < 4; ++r) wr[4 * (f) + r] = ldw(wpa[r], (slot) * 16384);
#define PHI(p, V) phi_step<V>(acc[p], pc2, pe, pm, pf)
#define TS(i)
#define X_J4 3
    if (__builtin_expect(jj < X_J4, 1)) {
    xq = (ROTX) * 128; xq = min(xq, xq - 896);
    sb = (jj < 2 ? 896 + ((b & 1) * 4 + t) * 112 + hd * 896 : ((b & 1) * 4 + (t & 1) * 2 + hd) * 112) + rh * 43008;
    xr = (R * 128 + w * 32) * 896;
    so[0] = xq * 1344 + sb; xs[0] = xq + xr;
    xo[0] = (lane >> 3) * 896 + ((lane & 7) ^ g) * 16;
    xo[1] = (xo[0] ^ 64) + 6144; xo[2] = xo[0] + 12288; xo[3] = xo[1] + 12288;
    { const u32 pc = (lane & 7) ^ ((g & 1) * 4); va[0] = (lane >> 3) * 2688 + (pc < 7 ? pc * 16 : OOB); va[1] = va[0] + 8 * 2688 - 1024; }
    const rsrc_t rw = mk_rsrc(wqkv, 448 * 2688);
    const rsrc_t rx = mk_rsrc(x, 2048 * 448 * 2);
        M0(xb, 0);
        NOP();
        dmao<0>(rx, xo[0], xs[0]);
        dmao<1024>(rx, xo[1], xs[0]);
        dmao<2048>(rx, xo[2], xs[0]);
        dmao<3072>(rx, xo[3], xs[0]);
        M0(wb, 0);
        NOP();
        dmao<0>(rw, va[0], so[0] + 0);
        dmao<1024>(rw, va[1], so[0] + 0);
        SETUP()
        SLAB(1)
        M0(xb, 16384);
        NOP();
        dmao<0>(rx, xo[0], xs[1]);
        dmao<1024>(rx, xo[1], xs[1]);
        SB(); LATE0() AUX0() AUX1() AUX2() SB();
        M0(wb, 8192);
        NOP();
        dmao<0>(rw, va[0], so[0] + 86016);
        dmao<1024>(rw, va[1], so[0] + 86016);
        SB(); AUX3() SB();
        M0(xb, 16384);
        NOP();
        dmao<2048>(rx, xo[2], xs[1]);
        dmao<3072>(rx, xo[3], xs[1]);
        M0(wb, 16384);
        NOP();
        dmao<0>(rw, va[0], so[1] + 0);
        dmao<1024>(rw, va[1], so[1] + 0);
        SLAB(2)
        M0(xb, 32768);
        NOP();
        dmao<0>(rx, xo[0], xs[2]);
        SLAB(3)
        SLAB(4)
        SLAB(5)
        SLAB(6)
    if (__builtin_expect(jj == 2, 0)) {
        u32 msk, qo;
#define PD 112
#define ACC1() acc[1][0] = acc[1][1] = acc[0][0];
#define AUX4() { msk = hh ? 0u : ~0u; qo = X_NOPQ * OOB + WS_PQ + (R * 4 + rh * 2) * 32768 + (((b & 1) * 2 + (t & 1)) * 4 + hd) * 2048 + lane * 16; PIN(qo); PIN(msk); }
#define MF(c, xa, wa) { c = MFMA32(wa, xa, c); PIN(c); }
#define QPUB(o, f) { st128(rws, qo + (o), f[0], 1); st128(rws, qo + (o) + 1024, (u32x4){f[1][0], f[1][1], f[1][2] & msk, f[1][3] & msk}, 1); }
#define PUB0() QPUB(0, pf[0])
#define PUB1() QPUB(32768, pf[1])
#define HINT0()
#define HINT1()
        SB(); ACC1() AUX4() SB();
        TAG()
        vmw<9>();
        BAR(); KEEP() TS(0)
        wf[0] = ldw2(wp, 0);
        xf[0][0] = ld<u32x4>(xp[0], 0); xf[0][1] = ld<u32x4>(xp[0], 4096);
        wf[1] = ldw2(wp, 2048);
        SB();
        MF(acc[0][0], xf[0][0], wf[0]);
        wf[2] = ldw2(wp, 4096);
        dmao<1024>(rx, xo[1], xs[2]);
        SB();
        MF(acc[0][1], xf[0][1], wf[0]);
        xf[1][0] = ld<u32x4>(xp[1], 0); xf[1][1] = ld<u32x4>(xp[1], 4096);
        SB();
        MF(acc[1][0], xf[0][0], wf[1]);
        M0(wb, 24576);
        wf[0] = ldw2(wp, 6144);
        dmao<0>(rw, va[0], so[1] + 86016);
        SB();
        MF(acc[1][1], xf[0][1], wf[1]);
        SB();
        MF(acc[0][0], xf[1][0], wf[2]);
        vmw<7>();
        BARN(0);
        TS(1)
        wf[1] = ldw2(wp, 8192);
        dmao<1024>(rw, va[1], so[1] + 86016);
        SB();
        MF(acc[0][1], xf[1][1], wf[2]);
        xf[2][0] = ld<u32x4>(xp[2], 0); xf[2][1] = ld<u32x4>(xp[2], 4096);
        SB();
        MF(acc[1][0], xf[1][0], wf[0]);
        M0(xb, 32768);
        wf[2] = ldw2(wp, 10240);
        dmao<2048>(rx, xo[2], xs[2]);
        SB();
        MF(acc[1][1], xf[1][1], wf[0]);
        SB();
        MF(acc[0][0], xf[2][0], wf[1]);
        wf[0] = ldw2(wp, 12288);
        dmao<3072>(rx, xo[3], xs[2]);
        SB();
        MF(acc[0][1], xf[2][1], wf[1]);
        xf[3][0] = ld<u32x4>(xp[3], 0); xf[3][1] = ld<u32x4>(xp[3], 4096);
        SB();
        MF(acc[1][0], xf[2][0], wf[2]);
        M0(wb, 0);
        wf[1] = ldw2(wp, 14336);
        dmao<0>(rw, va[0], so[2] + 0);
        SB();
        MF(acc[1][1], xf[2][1], wf[2]);
        SB();
        MF(acc[0][0], xf[3][0], wf[0]);
        vmw<7>();
        BARN(0);
        TS(2)
        wf[2] = ldw2(wp, 16384);
        dmao<1024>(rw, va[1], so[2] + 0);
        SB();
        MF(acc[0][1], xf[3][1], wf[0]);
        xf[4][0] = ld<u32x4>(xp[0], 16384); xf[4][1] = ld<u32x4>(xp[0], 20480);
        SB();
        MF(acc[1][0], xf[3][0], wf[1]);
        M0(xb, 0);
        wf[0] = ldw2(wp, 18432);
        dmao<0>(rx, xo[0], xs[3]);
        SB();
        MF(acc[1][1], xf[3][1], wf[1]);
        SB();
        MF(acc[0][0], xf[4][0], wf[2]);
        wf[1] = ldw2(wp, 20480);
        dmao<1024>(rx, xo[1], xs[3]);
        SB();
        MF(acc[0][1], xf[4][1], wf[2]);
        xf[5][0] = ld<u32x4>(xp[1], 16384); xf[5][1] = ld<u32x4>(xp[1], 20480);
        SB();
        MF(acc[1][0], xf[4][0], wf[0]);
        M0(wb, 8192);
        wf[2] = ldw2(wp, 22528);
        dmao<0>(rw, va[0], so[2] + 86016);
        SB();
        MF(acc[1][1], xf[4][1], wf[0]);
        SB();
        MF(acc[0][0], xf[5][0], wf[1]);
        vmw<7>();
        BARN(0);
        TS(3)
        wf[0] = ldw2(wp, 24576);
        dmao<1024>(rw, va[1], so[2] + 86016);
        SB();
        MF(acc[0][1], xf[5][1], wf[1]);
        xf[6][0] = ld<u32x4>(xp[2], 16384); xf[6][1] = ld<u32x4>(xp[2], 20480);
        SB();
        MF(acc[1][0], xf[5][0], wf[2]);
        M0(xb, 0);
        wf[1] = ldw2(wp, 26624);
        dmao<2048>(rx, xo[2], xs[3]);
        SB();
        MF(acc[1][1], xf[5][1], wf[2]);
        SB();
        MF(acc[0][0], xf[6][0], wf[0]);
        wf[2] = ldw2(wp, 28672);
        dmao<3072>(rx, xo[3], xs[3]);
        SB();
        MF(acc[0][1], xf[6][1], wf[0]);
        xf[7][0] = ld<u32x4>(xp[3], 16384); xf[7][1] = ld<u32x4>(xp[3], 20480);
        SB();
        MF(acc[1][0], xf[6][0], wf[1]);
        M0(wb, 16384);
        wf[0] = ldw2(wp, 30720);
        dmao<0>(rw, va[0], so[3] + 0);
        SB();
        MF(acc[1][1], xf[6][1], wf[1]);
        SB();
        MF(acc[0][0], xf[7][0], wf[2]);
        vmw<7>();
        BARN(0);
        TS(4)
        wf[1] = ldw2(wp, 0);
        dmao<1024>(rw, va[1], so[3] + 0);
        SB();
        MF(acc[0][1], xf[7][1], wf[2]);
        xf[0][0] = ld<u32x4>(xp[0], 32768); xf[0][1] = ld<u32x4>(xp[0], 36864);
        SB();
        MF(acc[1][0], xf[7][0], wf[0]);
        M0(xb, 16384);
        wf[2] = ldw2(wp, 2048);
        dmao<0>(rx, xo[0], xs[4]);
        SB();
        MF(acc[1][1], xf[7][1], wf[0]);
        SB();
        MF(acc[0][0], xf[0][0], wf[1]);
        wf[0] = ldw2(wp, 4096);
        dmao<1024>(rx, xo[1], xs[4]);
        SB();
        MF(acc[0][1], xf[0][1], wf[1]);
        xf[1][0] = ld<u32x4>(xp[1], 32768); xf[1][1] = ld<u32x4>(xp[1], 36864);
        SB();
        MF(acc[1][0], xf[0][0], wf[2]);
        M0(wb, 24576);
        wf[1] = ldw2(wp, 6144);
        dmao<0>(rw, va[0], so[3] + 86016);
        SB();
        MF(acc[1][1], xf[0][1], wf[2]);
        SB();
        MF(acc[0][0], xf[1][0], wf[0]);
        vmw<7>();
        BARN(0);
        TS(5)
        wf[2] = ldw2(wp, 8192);
        dmao<1024>(rw, va[1], so[3] + 86016);
        SB();
        MF(acc[0][1], xf[1][1], wf[0]);
        xf[2][0] = ld<u32x4>(xp[2], 32768); xf[2][1] = ld<u32x4>(xp[2], 36864);
        SB();
        MF(acc[1][0], xf[1][0], wf[1]);
        M0(xb, 16384);
        wf[0] = ldw2(wp, 10240);
        dmao<2048>(rx, xo[2], xs[4]);
        SB();
        MF(acc[1][1], xf[1][1], wf[1]);
        SB();
        MF(acc[0][0], xf[2][0], wf[2]);
        wf[1] = ldw2(wp, 12288);
        dmao<3072>(rx, xo[3], xs[4]);
        SB();
        MF(acc[0][1], xf[2][1], wf[2]);
        xf[3][0] = ld<u32x4>(xp[3], 32768); xf[3][1] = ld<u32x4>(xp[3], 36864);
        SB();
        MF(acc[1][0], xf[2][0], wf[0]);
        M0(wb, 0);
        wf[2] = ldw2(wp, 14336);
        dmao<0>(rw, va[0], so[4] + 0);
        SB();
        MF(acc[1][1], xf[2][1], wf[0]);
        SB();
        MF(acc[0][0], xf[3][0], wf[1]);
        vmw<7>();
        BARN(0);
        TS(6)
        wf[0] = ldw2(wp, 16384);
        dmao<1024>(rw, va[1], so[4] + 0);
        SB();
        MF(acc[0][1], xf[3][1], wf[1]);
        xf[4][0] = ld<u32x4>(xp[0], 0); xf[4][1] = ld<u32x4>(xp[0], 4096);
        SB();
        MF(acc[1][0], xf[3][0], wf[2]);
        M0(xb, 32768);
        wf[1] = ldw2(wp, 18432);
        dmao<0>(rx, xo[0], xs[5]);
        SB();
        MF(acc[1][1], xf[3][1], wf[2]);
        SB();
        MF(acc[0][0], xf[4][0], wf[0]);
        wf[2] = ldw2(wp, 20480);
        dmao<1024>(rx, xo[1], xs[5]);
        SB();
        MF(acc[0][1], xf[4][1], wf[0]);
        xf[5][0] = ld<u32x4>(xp[1], 0); xf[5][1] = ld<u32x4>(xp[1], 4096);
        SB();
        MF(acc[1][0], xf[4][0], wf[1]);
        M0(wb, 8192);
        wf[0] = ldw2(wp, 22528);
        dmao<0>(rw, va[0], so[4] + 86016);
        SB();
        MF(acc[1][1], xf[4][1], wf[1]);
        SB();
        MF(acc[0][0], xf[5][0], wf[2]);
        vmw<7>();
        BARN(0);
        TS(7)
        wf[1] = ldw2(wp, 24576);
        dmao<1024>(rw, va[1], so[4] + 86016);
        SB();
        MF(acc[0][1], xf[5][1], wf[2]);
        xf[6][0] = ld<u32x4>(xp[2], 0); xf[6][1] = ld<u32x4>(xp[2], 4096);
        SB();
        MF(acc[1][0], xf[5][0], wf[0]);
        M0(xb, 32768);
        wf[2] = ldw2(wp, 26624);
        dmao<2048>(rx, xo[2], xs[5]);
        SB();
        MF(acc[1][1], xf[5][1], wf[0]);
        SB();
        MF(acc[0][0], xf[6][0], wf[1]);
        wf[0] = ldw2(wp, 28672);
        SB();
        MF(acc[0][1], xf[6][1], wf[1]);
        xf[7][0] = ld<u32x4>(xp[3], 0); xf[7][1] = ld<u32x4>(xp[3], 4096);
        SB();
        MF(acc[1][0], xf[6][0], wf[2]);
        wf[1] = ldw2(wp, 30720);
        SB();
        MF(acc[1][1], xf[6][1], wf[2]);
        SB();
        MF(acc[0][0], xf[7][0], wf[0]);
        vmw<5>();
        BARN(0);
        TS(8)
        M0(xb, 0);
        wf[2] = ldw2(wp, 0);
        dmao<0>(rx, xo[0], xs[6]);
        SB();
        MF(acc[0][1], xf[7][1], wf[0]);
        M0(xb, 32768);
        xf[0][0] = ld<u32x4>(xp[0], 16384); xf[0][1] = ld<u32x4>(xp[0], 20480);
        dmao<3072>(rx, xo[3], xs[5]);
        SB();
        MF(acc[1][0], xf[7][0], wf[1]);
        M0(xb, 0);
        wf[0] = ldw2(wp, 2048);
        dmao<1024>(rx, xo[1], xs[6]);
        SB();
        MF(acc[1][1], xf[7][1], wf[1]);
        M0(wb, 16384);
        NOP();
        dmao<0>(rw, va[0], so[5]);
        SB();
        MF(acc[0][0], xf[0][0], wf[2]);
        wf[1] = ldw2(wp, 4096);
        dmao<1024>(rw, va[1], so[5]);
        SB();
        MF(acc[0][1], xf[0][1], wf[2]);
        M0(xb, 0);
        xf[1][0] = ld<u32x4>(xp[1], 16384); xf[1][1] = ld<u32x4>(xp[1], 20480);
        dmao<2048>(rx, xo[2], xs[6]);
        SB();
        MF(acc[1][0], xf[0][0], wf[0]);
        wf[2] = ldw2(wp, 6144);
        dmao<3072>(rx, xo[3], xs[6]);
        SB();
        MF(acc[1][1], xf[0][1], wf[0]);
        M0(wb, 24576);
        NOP();
        dmao<0>(rw, va[0], so[6]);
        SB();
        MF(acc[0][0], xf[1][0], wf[1]);
        vmw<9>();
        BARN(0);
        TS(9)
        wf[0] = ldw2(wp, 8192);
        dmao<1024>(rw, va[1], so[6]);
        SB();
        MF(acc[0][1], xf[1][1], wf[1]);
        xf[2][0] = ld<u32x4>(xp[2], 16384); xf[2][1] = ld<u32x4>(xp[2], 20480);
        SB();
        MF(acc[1][0], xf[1][0], wf[2]);
        wf[1] = ldw2(wp, 10240);
        SB();
        MF(acc[1][1], xf[1][1], wf[2]);
        SB();
        MF(acc[0][0], xf[2][0], wf[0]);
        wf[2] = ldw2(wp, 12288);
        SB();
        MF(acc[0][1], xf[2][1], wf[0]);
        xf[3][0] = ld<u32x4>(xp[3], 16384); xf[3][1] = ld<u32x4>(xp[3], 20480);
        SB();
        MF(acc[1][0], xf[2][0], wf[1]);
        M0(wb, 0);
        wf[0] = ldw2(wp, 14336);
        dmao<0>(rw, va[0], so[5] + PD);
        SB();
        MF(acc[1][1], xf[2][1], wf[1]);
        SB();
        MF(acc[0][0], xf[3][0], wf[2]);
        vmw<5>();
        BARN(0);
        TS(10)
        wf[1] = ldw2(wp, 16384);
        dmao<1024>(rw, va[1], so[5] + PD);
        SB();
        MF(acc[0][1], xf[3][1], wf[2]);
        xf[4][0] = ld<u32x4>(xp[0], 32768); xf[4][1] = ld<u32x4>(xp[0], 36864);
        SB();
        MF(acc[1][0], xf[3][0], wf[0]);
        wf[2] = ldw2(wp, 18432);
        SB();
        MF(acc[1][1], xf[3][1], wf[0]);
        xf[5][0] = ld<u32x4>(xp[1], 32768); xf[5][1] = ld<u32x4>(xp[1], 36864);
        SB();
        MF(acc[0][0], xf[4][0], wf[1]);
        wf[0] = ldw2(wp, 20480);
        SB();
        MF(acc[0][1], xf[4][1], wf[1]);
        xf[6][0] = ld<u32x4>(xp[2], 32768); xf[6][1] = ld<u32x4>(xp[2], 36864);
        SB();
        MF(acc[0][0], xf[5][0], wf[2]);
        M0(wb, 8192);
        wf[1] = ldw2(wp, 22528);
        dmao<0>(rw, va[0], so[6] + PD);
        SB();
        MF(acc[0][1], xf[5][1], wf[2]);
        xf[7][0] = ld<u32x4>(xp[3], 32768); xf[7][1] = ld<u32x4>(xp[3], 36864);
        SB();
        MF(acc[0][0], xf[6][0], wf[0]);
        vmw<3>();
        BARN(2);
        TS(11)
        wf[2] = ldw2(wp, 24576);
        dmao<1024>(rw, va[1], so[6] + PD);
        SB();
        MF(acc[0][1], xf[6][1], wf[0]);
        xf[0][0] = ld<u32x4>(xp[0], 0); xf[0][1] = ld<u32x4>(xp[0], 4096);
        SB();
        MF(acc[0][0], xf[7][0], wf[1]);
        wf[0] = ldw2(wp, 26624);
        SB();
        MF(acc[0][1], xf[7][1], wf[1]);
        xf[1][0] = ld<u32x4>(xp[1], 0); xf[1][1] = ld<u32x4>(xp[1], 4096);
        SB();
        MF(acc[0][0], xf[0][0], wf[2]);
        M0(xb, 16384);
        wf[1] = ldw2(wp, 28672);
        dmao<0>(ro, wv[0], wq[0]);
        SB();
        MF(acc[0][1], xf[0][1], wf[2]);
        xf[2][0] = ld<u32x4>(xp[2], 0); xf[2][1] = ld<u32x4>(xp[2], 4096);
        SB();
        MF(acc[0][0], xf[1][0], wf[0]);
        wf[2] = ldw2(wp, 30720);
        dmao<1024>(ro, wv[0], wq[1]);
        SB();
        MF(acc[0][1], xf[1][1], wf[0]);
        xf[3][0] = ld<u32x4>(xp[3], 0); xf[3][1] = ld<u32x4>(xp[3], 4096);
        SB();
        MF(acc[0][0], xf[2][0], wf[1]);
        vmw<4>();
        BARN(2);
        TS(12)
        HINT0()
        wf[0] = ldw2(wp, 0);
        dmao<2048>(ro, wv[0], wq[2]);
        SB();
        MF(acc[0][1], xf[2][1], wf[1]);
        SB();
        MF(acc[0][0], xf[3][0], wf[2]);
        wf[1] = ldw2(wp, 2048);
        dmao<3072>(ro, wv[0], wq[3]);
        SB();
        MF(acc[0][1], xf[3][1], wf[2]);
        SB();
        MF(acc[1][0], xf[4][0], wf[0]);
        M0(xb, 32768);
        wf[2] = ldw2(wp, 4096);
        dmao<0>(ro, wv[1], wq[0]);
        SB();
        MF(acc[1][1], xf[4][1], wf[0]);
        PHI(0, 0);
        PHI(0, 1);
        SB();
        MF(acc[1][0], xf[5][0], wf[1]);
        PHI(0, 2);
        PHI(0, 3);
        wf[0] = ldw2(wp, 6144);
        dmao<1024>(ro, wv[1], wq[1]);
        SB();
        MF(acc[1][1], xf[5][1], wf[1]);
        PHI(0, 4);
        PHI(0, 5);
        SB();
        MF(acc[1][0], xf[6][0], wf[2]);
        PHI(0, 6);
        PHI(0, 7);
        vmw<6>();
        BARN(0);
        TS(13)
        HINT1()
        wf[1] = ldw2(wp, 8192);
        dmao<2048>(ro, wv[1], wq[2]);
        SB();
        MF(acc[1][1], xf[6][1], wf[2]);
        PHI(0, 8);
        PHI(0, 9);
        SB();
        MF(acc[1][0], xf[7][0], wf[0]);
        PHI(0, 10);
        PHI(0, 11);
        wf[2] = ldw2(wp, 10240);
        dmao<3072>(ro, wv[1], wq[3]);
        SB();
        MF(acc[1][1], xf[7][1], wf[0]);
        PHI(0, 12);
        PHI(0, 13);
        SB();
        MF(acc[1][0], xf[0][0], wf[1]);
        PHI(0, 14);
        PHI(0, 15);
        wf[0] = ldw2(wp, 12288);
        SB();
        MF(acc[1][1], xf[0][1], wf[1]);
        PHI(0, 16);
        PHI(0, 17);
        PUB0()
        SB();
        MF(acc[1][0], xf[1][0], wf[2]);
        PHI(0, 18);
        PHI(0, 19);
        wf[1] = ldw2(wp, 14336);
        SB();
        MF(acc[1][1], xf[1][1], wf[2]);
        PHI(0, 20);
        PHI(0, 21);
        SB();
        MF(acc[1][0], xf[2][0], wf[0]);
        PHI(0, 22);
        PHI(0, 23);
        SB();
        MF(acc[1][1], xf[2][1], wf[0]);
        PHI(0, 24);
        PHI(0, 25);
        SB();
        MF(acc[1][0], xf[3][0], wf[1]);
        PHI(0, 26);
        PHI(0, 27);
        SB();
        MF(acc[1][1], xf[3][1], wf[1]);
        PHI(0, 28);
        PHI(0, 29);
        SB();
        PHI(0, 30);
        PHI(0, 31);
        PHI(0, 32);
        PHI(0, 33);
        PUB1()
        TS(14)
        #define O_YOUNG 4
#undef PD
#undef ACC1
#undef AUX4
#undef MF
#undef PUB0
#undef PUB1
#undef HINT0
#undef HINT1
        phi_run<0, 18>(acc[1], pc2, pe, pm, pf);
        QPUB(4096, pf[0])
        phi_run<18, 34>(acc[1], pc2, pe, pm, pf);
        QPUB(32768 + 4096, pf[1])
        SB();
        vmw<0>();
        __builtin_amdgcn_raw_buffer_store_b64((u32x2){tag, tag}, rws, lane ? OOB : 0, WS_QF + ((R * 2 + rh) * 4 + (b & 1) * 2 + (t & 1)) * 128 + hd * 8, 16);
#undef O_YOUNG
        BAR();
        wv[3] |= (lane ^ (lane >> 2)) & 8 ? OOB : 0;
        WO4(2, 0) WO2(3, 0, 1)
        WRD(0, 1) WRD(2, 2)
#pragma unroll
        for (int r = 0; r < 4; ++r) { const lptr p = lp((u32)(size_t)wpa[r] ^ 128); wr[4 + r] = ldw(p, 16384); wr[12 + r] = ldw(p, 32768); }
#undef QPUB
    } else {
        const int head = (b & 1) * 8 + 2 * t + hd;
        u32 po, ho;
        lptr mw, mr;
        u64 th;
#define PD 896
#define ACC1() acc[1][0] = splat16(n >= 28 ? 1.f : 0.f); PIN(acc[1][0]); acc[1][1] = acc[1][0];
#define AUX4() { po = WS_PART + (u32)(head * 16 + rh * 8 + hh * 4 + (n >> 3)) * PT_S + R * 256 + (((n >> 1) & 3) + (n & 1) * 4) * 16; PIN(po); \
        ho = lane < NH1 && !rh ? WS_H1 + (u32)(head * NH1 + lane) * 128 + R * 8 : OOB; PIN(ho); \
        mw = lp(A_W + 16384 + (w ^ 2) * 2048 + lane * 16); mr = lp(A_W + 16384 + w * 2048 + lane * 16); PIN(mw); PIN(mr); }
#define MF(c, xa, wa) { c = MFMA32(xa, wa, c); PIN(c); }
#define PUB1()
#define HINTA() asm volatile("s_memrealtime %0" : "=s"(th) :: "memory");
#define HINTB() { asm volatile("s_waitcnt lgkmcnt(0)" : "+s"(th) :: "memory"); __builtin_amdgcn_raw_buffer_store_b64((u32x2){(u32)th, tag}, rws, ho, 0, 16); }
#define PUB0()
#define HINT0() HINTA()
#define HINT1() HINTB()
        SB(); ACC1() AUX4() SB();
        TAG()
        vmw<9>();
        BAR(); KEEP() TS(0)
        wf[0] = ldw2(wp, 0);
        xf[0][0] = ld<u32x4>(xp[0], 0); xf[0][1] = ld<u32x4>(xp[0], 4096);
        wf[1] = ldw2(wp, 2048);
        SB();
        MF(acc[0][0], xf[0][0], wf[0]);
        wf[2] = ldw2(wp, 4096);
        dmao<1024>(rx, xo[1], xs[2]);
        SB();
        MF(acc[0][1], xf[0][1], wf[0]);
        xf[1][0] = ld<u32x4>(xp[1], 0); xf[1][1] = ld<u32x4>(xp[1], 4096);
        SB();
        MF(acc[1][0], xf[0][0], wf[1]);
        M0(wb, 24576);
        wf[0] = ldw2(wp, 6144);
        dmao<0>(rw, va[0], so[1] + 86016);
        SB();
        MF(acc[1][1], xf[0][1], wf[1]);
        SB();
        MF(acc[0][0], xf[1][0], wf[2]);
        vmw<7>();
        BARN(0);
        TS(1)
        wf[1] = ldw2(wp, 8192);
        dmao<1024>(rw, va[1], so[1] + 86016);
        SB();
        MF(acc[0][1], xf[1][1], wf[2]);
        xf[2][0] = ld<u32x4>(xp[2], 0); xf[2][1] = ld<u32x4>(xp[2], 4096);
        SB();
        MF(acc[1][0], xf[1][0], wf[0]);
        M0(xb, 32768);
        wf[2] = ldw2(wp, 10240);
        dmao<2048>(rx, xo[2], xs[2]);
        SB();
        MF(acc[1][1], xf[1][1], wf[0]);
        SB();
        MF(acc[0][0], xf[2][0], wf[1]);
        wf[0] = ldw2(wp, 12288);
        dmao<3072>(rx, xo[3], xs[2]);
        SB();
        MF(acc[0][1], xf[2][1], wf[1]);
        xf[3][0] = ld<u32x4>(xp[3], 0); xf[3][1] = ld<u32x4>(xp[3], 4096);
        SB();
        MF(acc[1][0], xf[2][0], wf[2]);
        M0(wb, 0);
        wf[1] = ldw2(wp, 14336);
        dmao<0>(rw, va[0], so[2] + 0);
        SB();
        MF(acc[1][1], xf[2][1], wf[2]);
        SB();
        MF(acc[0][0], xf[3][0], wf[0]);
        vmw<7>();
        BARN(0);
        TS(2)
        wf[2] = ldw2(wp, 16384);
        dmao<1024>(rw, va[1], so[2] + 0);
        SB();
        MF(acc[0][1], xf[3][1], wf[0]);
        xf[4][0] = ld<u32x4>(xp[0], 16384); xf[4][1] = ld<u32x4>(xp[0], 20480);
        SB();
        MF(acc[1][0], xf[3][0], wf[1]);
        M0(xb, 0);
        wf[0] = ldw2(wp, 18432);
        dmao<0>(rx, xo[0], xs[3]);
        SB();
        MF(acc[1][1], xf[3][1], wf[1]);
        SB();
        MF(acc[0][0], xf[4][0], wf[2]);
        wf[1] = ldw2(wp, 20480);
        dmao<1024>(rx, xo[1], xs[3]);
        SB();
        MF(acc[0][1], xf[4][1], wf[2]);
        xf[5][0] = ld<u32x4>(xp[1], 16384); xf[5][1] = ld<u32x4>(xp[1], 20480);
        SB();
        MF(acc[1][0], xf[4][0], wf[0]);
        M0(wb, 8192);
        wf[2] = ldw2(wp, 22528);
        dmao<0>(rw, va[0], so[2] + 86016);
        SB();
        MF(acc[1][1], xf[4][1], wf[0]);
        SB();
        MF(acc[0][0], xf[5][0], wf[1]);
        vmw<7>();
        BARN(0);
        TS(3)
        wf[0] = ldw2(wp, 24576);
        dmao<1024>(rw, va[1], so[2] + 86016);
        SB();
        MF(acc[0][1], xf[5][1], wf[1]);
        xf[6][0] = ld<u32x4>(xp[2], 16384); xf[6][1] = ld<u32x4>(xp[2], 20480);
        SB();
        MF(acc[1][0], xf[5][0], wf[2]);
        M0(xb, 0);
        wf[1] = ldw2(wp, 26624);
        dmao<2048>(rx, xo[2], xs[3]);
        SB();
        MF(acc[1][1], xf[5][1], wf[2]);
        SB();
        MF(acc[0][0], xf[6][0], wf[0]);
        wf[2] = ldw2(wp, 28672);
        dmao<3072>(rx, xo[3], xs[3]);
        SB();
        MF(acc[0][1], xf[6][1], wf[0]);
        xf[7][0] = ld<u32x4>(xp[3], 16384); xf[7][1] = ld<u32x4>(xp[3], 20480);
        SB();
        MF(acc[1][0], xf[6][0], wf[1]);
        M0(wb, 16384);
        wf[0] = ldw2(wp, 30720);
        dmao<0>(rw, va[0], so[3] + 0);
        SB();
        MF(acc[1][1], xf[6][1], wf[1]);
        SB();
        MF(acc[0][0], xf[7][0], wf[2]);
        vmw<7>();
        BARN(0);
        TS(4)
        wf[1] = ldw2(wp, 0);
        dmao<1024>(rw, va[1], so[3] + 0);
        SB();
        MF(acc[0][1], xf[7][1], wf[2]);
        xf[0][0] = ld<u32x4>(xp[0], 32768); xf[0][1] = ld<u32x4>(xp[0], 36864);
        SB();
        MF(acc[1][0], xf[7][0], wf[0]);
        M0(xb, 16384);
        wf[2] = ldw2(wp, 2048);
        dmao<0>(rx, xo[0], xs[4]);
        SB();
        MF(acc[1][1], xf[7][1], wf[0]);
        SB();
        MF(acc[0][0], xf[0][0], wf[1]);
        wf[0] = ldw2(wp, 4096);
        dmao<1024>(rx, xo[1], xs[4]);
        SB();
        MF(acc[0][1], xf[0][1], wf[1]);
        xf[1][0] = ld<u32x4>(xp[1], 32768); xf[1][1] = ld<u32x4>(xp[1], 36864);
        SB();
        MF(acc[1][0], xf[0][0], wf[2]);
        M0(wb, 24576);
        wf[1] = ldw2(wp, 6144);
        dmao<0>(rw, va[0], so[3] + 86016);
        SB();
        MF(acc[1][1], xf[0][1], wf[2]);
        SB();
        MF(acc[0][0], xf[1][0], wf[0]);
        vmw<7>();
        BARN(0);
        TS(5)
        wf[2] = ldw2(wp, 8192);
        dmao<1024>(rw, va[1], so[3] + 86016);
        SB();
        MF(acc[0][1], xf[1][1], wf[0]);
        xf[2][0] = ld<u32x4>(xp[2], 32768); xf[2][1] = ld<u32x4>(xp[2], 36864);
        SB();
        MF(acc[1][0], xf[1][0], wf[1]);
        M0(xb, 16384);
        wf[0] = ldw2(wp, 10240);
        dmao<2048>(rx, xo[2], xs[4]);
        SB();
        MF(acc[1][1], xf[1][1], wf[1]);
        SB();
        MF(acc[0][0], xf[2][0], wf[2]);
        wf[1] = ldw2(wp, 12288);
        dmao<3072>(rx, xo[3], xs[4]);
        SB();
        MF(acc[0][1], xf[2][1], wf[2]);
        xf[3][0] = ld<u32x4>(xp[3], 32768); xf[3][1] = ld<u32x4>(xp[3], 36864);
        SB();
        MF(acc[1][0], xf[2][0], wf[0]);
        M0(wb, 0);
        wf[2] = ldw2(wp, 14336);
        dmao<0>(rw, va[0], so[4] + 0);
        SB();
        MF(acc[1][1], xf[2][1], wf[0]);
        SB();
        MF(acc[0][0], xf[3][0], wf[1]);
        vmw<7>();
        BARN(0);
        TS(6)
        wf[0] = ldw2(wp, 16384);
        dmao<1024>(rw, va[1], so[4] + 0);
        SB();
        MF(acc[0][1], xf[3][1], wf[1]);
        xf[4][0] = ld<u32x4>(xp[0], 0); xf[4][1] = ld<u32x4>(xp[0], 4096);
        SB();
        MF(acc[1][0], xf[3][0], wf[2]);
        M0(xb, 32768);
        wf[1] = ldw2(wp, 18432);
        dmao<0>(rx, xo[0], xs[5]);
        SB();
        MF(acc[1][1], xf[3][1], wf[2]);
        SB();
        MF(acc[0][0], xf[4][0], wf[0]);
        wf[2] = ldw2(wp, 20480);
        dmao<1024>(rx, xo[1], xs[5]);
        SB();
        MF(acc[0][1], xf[4][1], wf[0]);
        xf[5][0] = ld<u32x4>(xp[1], 0); xf[5][1] = ld<u32x4>(xp[1], 4096);
        SB();
        MF(acc[1][0], xf[4][0], wf[1]);
        M0(wb, 8192);
        wf[0] = ldw2(wp, 22528);
        dmao<0>(rw, va[0], so[4] + 86016);
        SB();
        MF(acc[1][1], xf[4][1], wf[1]);
        SB();
        MF(acc[0][0], xf[5][0], wf[2]);
        vmw<7>();
        BARN(0);
        TS(7)
        wf[1] = ldw2(wp, 24576);
        dmao<1024>(rw, va[1], so[4] + 86016);
        SB();
        MF(acc[0][1], xf[5][1], wf[2]);
        xf[6][0] = ld<u32x4>(xp[2], 0); xf[6][1] = ld<u32x4>(xp[2], 4096);
        SB();
        MF(acc[1][0], xf[5][0], wf[0]);
        M0(xb, 32768);
        wf[2] = ldw2(wp, 26624);
        dmao<2048>(rx, xo[2], xs[5]);
        SB();
        MF(acc[1][1], xf[5][1], wf[0]);
        SB();
        MF(acc[0][0], xf[6][0], wf[1]);
        wf[0] = ldw2(wp, 28672);
        SB();
        MF(acc[0][1], xf[6][1], wf[1]);
        xf[7][0] = ld<u32x4>(xp[3], 0); xf[7][1] = ld<u32x4>(xp[3], 4096);
        SB();
        MF(acc[1][0], xf[6][0], wf[2]);
        wf[1] = ldw2(wp, 30720);
        SB();
        MF(acc[1][1], xf[6][1], wf[2]);
        SB();
        MF(acc[0][0], xf[7][0], wf[0]);
        vmw<5>();
        BARN(0);
        TS(8)
        M0(xb, 0);
        wf[2] = ldw2(wp, 0);
        dmao<0>(rx, xo[0], xs[6]);
        SB();
        MF(acc[0][1], xf[7][1], wf[0]);
        M0(xb, 32768);
        xf[0][0] = ld<u32x4>(xp[0], 16384); xf[0][1] = ld<u32x4>(xp[0], 20480);
        dmao<3072>(rx, xo[3], xs[5]);
        SB();
        MF(acc[1][0], xf[7][0], wf[1]);
        M0(xb, 0);
        wf[0] = ldw2(wp, 2048);
        dmao<1024>(rx, xo[1], xs[6]);
        SB();
        MF(acc[1][1], xf[7][1], wf[1]);
        M0(wb, 16384);
        NOP();
        dmao<0>(rw, va[0], so[5]);
        SB();
        MF(acc[0][0], xf[0][0], wf[2]);
        wf[1] = ldw2(wp, 4096);
        dmao<1024>(rw, va[1], so[5]);
        SB();
        MF(acc[0][1], xf[0][1], wf[2]);
        M0(xb, 0);
        xf[1][0] = ld<u32x4>(xp[1], 16384); xf[1][1] = ld<u32x4>(xp[1], 20480);
        dmao<2048>(rx, xo[2], xs[6]);
        SB();
        MF(acc[1][0], xf[0][0], wf[0]);
        wf[2] = ldw2(wp, 6144);
        dmao<3072>(rx, xo[3], xs[6]);
        SB();
        MF(acc[1][1], xf[0][1], wf[0]);
        M0(wb, 24576);
        NOP();
        dmao<0>(rw, va[0], so[6]);
        SB();
        MF(acc[0][0], xf[1][0], wf[1]);
        vmw<9>();
        BARN(0);
        TS(9)
        wf[0] = ldw2(wp, 8192);
        dmao<1024>(rw, va[1], so[6]);
        SB();
        MF(acc[0][1], xf[1][1], wf[1]);
        xf[2][0] = ld<u32x4>(xp[2], 16384); xf[2][1] = ld<u32x4>(xp[2], 20480);
        SB();
        MF(acc[1][0], xf[1][0], wf[2]);
        wf[1] = ldw2(wp, 10240);
        SB();
        MF(acc[1][1], xf[1][1], wf[2]);
        SB();
        MF(acc[0][0], xf[2][0], wf[0]);
        wf[2] = ldw2(wp, 12288);
        SB();
        MF(acc[0][1], xf[2][1], wf[0]);
        xf[3][0] = ld<u32x4>(xp[3], 16384); xf[3][1] = ld<u32x4>(xp[3], 20480);
        SB();
        MF(acc[1][0], xf[2][0], wf[1]);
        M0(wb, 0);
        wf[0] = ldw2(wp, 14336);
        dmao<0>(rw, va[0], so[5] + PD);
        SB();
        MF(acc[1][1], xf[2][1], wf[1]);
        SB();
        MF(acc[0][0], xf[3][0], wf[2]);
        vmw<5>();
        BARN(0);
        TS(10)
        wf[1] = ldw2(wp, 16384);
        dmao<1024>(rw, va[1], so[5] + PD);
        SB();
        MF(acc[0][1], xf[3][1], wf[2]);
        xf[4][0] = ld<u32x4>(xp[0], 32768); xf[4][1] = ld<u32x4>(xp[0], 36864);
        SB();
        MF(acc[1][0], xf[3][0], wf[0]);
        wf[2] = ldw2(wp, 18432);
        SB();
        MF(acc[1][1], xf[3][1], wf[0]);
        xf[5][0] = ld<u32x4>(xp[1], 32768); xf[5][1] = ld<u32x4>(xp[1], 36864);
        SB();
        MF(acc[0][0], xf[4][0], wf[1]);
        wf[0] = ldw2(wp, 20480);
        SB();
        MF(acc[0][1], xf[4][1], wf[1]);
        xf[6][0] = ld<u32x4>(xp[2], 32768); xf[6][1] = ld<u32x4>(xp[2], 36864);
        SB();
        MF(acc[0][0], xf[5][0], wf[2]);
        M0(wb, 8192);
        wf[1] = ldw2(wp, 22528);
        dmao<0>(rw, va[0], so[6] + PD);
        SB();
        MF(acc[0][1], xf[5][1], wf[2]);
        xf[7][0] = ld<u32x4>(xp[3], 32768); xf[7][1] = ld<u32x4>(xp[3], 36864);
        SB();
        MF(acc[0][0], xf[6][0], wf[0]);
        vmw<3>();
        BARN(2);
        TS(11)
        wf[2] = ldw2(wp, 24576);
        dmao<1024>(rw, va[1], so[6] + PD);
        SB();
        MF(acc[0][1], xf[6][1], wf[0]);
        xf[0][0] = ld<u32x4>(xp[0], 0); xf[0][1] = ld<u32x4>(xp[0], 4096);
        SB();
        MF(acc[0][0], xf[7][0], wf[1]);
        wf[0] = ldw2(wp, 26624);
        SB();
        MF(acc[0][1], xf[7][1], wf[1]);
        xf[1][0] = ld<u32x4>(xp[1], 0); xf[1][1] = ld<u32x4>(xp[1], 4096);
        SB();
        MF(acc[0][0], xf[0][0], wf[2]);
        M0(xb, 16384);
        wf[1] = ldw2(wp, 28672);
        dmao<0>(ro, wv[0], wq[0]);
        SB();
        MF(acc[0][1], xf[0][1], wf[2]);
        xf[2][0] = ld<u32x4>(xp[2], 0); xf[2][1] = ld<u32x4>(xp[2], 4096);
        SB();
        MF(acc[0][0], xf[1][0], wf[0]);
        wf[2] = ldw2(wp, 30720);
        dmao<1024>(ro, wv[0], wq[1]);
        SB();
        MF(acc[0][1], xf[1][1], wf[0]);
        xf[3][0] = ld<u32x4>(xp[3], 0); xf[3][1] = ld<u32x4>(xp[3], 4096);
        SB();
        MF(acc[0][0], xf[2][0], wf[1]);
        vmw<4>();
        BARN(2);
        TS(12)
        HINT0()
        wf[0] = ldw2(wp, 0);
        dmao<2048>(ro, wv[0], wq[2]);
        SB();
        MF(acc[0][1], xf[2][1], wf[1]);
        SB();
        MF(acc[0][0], xf[3][0], wf[2]);
        wf[1] = ldw2(wp, 2048);
        dmao<3072>(ro, wv[0], wq[3]);
        SB();
        MF(acc[0][1], xf[3][1], wf[2]);
        SB();
        MF(acc[1][0], xf[4][0], wf[0]);
        M0(xb, 32768);
        wf[2] = ldw2(wp, 4096);
        dmao<0>(ro, wv[1], wq[0]);
        SB();
        MF(acc[1][1], xf[4][1], wf[0]);
        PHI(0, 0);
        PHI(0, 1);
        SB();
        MF(acc[1][0], xf[5][0], wf[1]);
        PHI(0, 2);
        PHI(0, 3);
        wf[0] = ldw2(wp, 6144);
        dmao<1024>(ro, wv[1], wq[1]);
        SB();
        MF(acc[1][1], xf[5][1], wf[1]);
        PHI(0, 4);
        PHI(0, 5);
        SB();
        MF(acc[1][0], xf[6][0], wf[2]);
        PHI(0, 6);
        PHI(0, 7);
        vmw<6>();
        BARN(0);
        TS(13)
        HINT1()
        wf[1] = ldw2(wp, 8192);
        dmao<2048>(ro, wv[1], wq[2]);
        SB();
        MF(acc[1][1], xf[6][1], wf[2]);
        PHI(0, 8);
        PHI(0, 9);
        SB();
        MF(acc[1][0], xf[7][0], wf[0]);
        PHI(0, 10);
        PHI(0, 11);
        wf[2] = ldw2(wp, 10240);
        dmao<3072>(ro, wv[1], wq[3]);
        SB();
        MF(acc[1][1], xf[7][1], wf[0]);
        PHI(0, 12);
        PHI(0, 13);
        SB();
        MF(acc[1][0], xf[0][0], wf[1]);
        PHI(0, 14);
        PHI(0, 15);
        wf[0] = ldw2(wp, 12288);
        SB();
        MF(acc[1][1], xf[0][1], wf[1]);
        PHI(0, 16);
        PHI(0, 17);
        PUB0()
        SB();
        MF(acc[1][0], xf[1][0], wf[2]);
        PHI(0, 18);
        PHI(0, 19);
        wf[1] = ldw2(wp, 14336);
        SB();
        MF(acc[1][1], xf[1][1], wf[2]);
        PHI(0, 20);
        PHI(0, 21);
        SB();
        MF(acc[1][0], xf[2][0], wf[0]);
        PHI(0, 22);
        PHI(0, 23);
        SB();
        MF(acc[1][1], xf[2][1], wf[0]);
        PHI(0, 24);
        PHI(0, 25);
        SB();
        MF(acc[1][0], xf[3][0], wf[1]);
        PHI(0, 26);
        PHI(0, 27);
        SB();
        MF(acc[1][1], xf[3][1], wf[1]);
        PHI(0, 28);
        PHI(0, 29);
        SB();
        PHI(0, 30);
        PHI(0, 31);
        PHI(0, 32);
        PHI(0, 33);
        PUB1()
        TS(14)
        #define O_YOUNG 0
#undef MF
        f32x16 d = splat16(0.f);
#pragma unroll
        for (int rt = 0; rt < 2; ++rt)
#pragma unroll
            for (int m = 0; m < 2; ++m) {
                u32x4 vf;
#pragma unroll
                for (int q = 0; q < 4; ++q) vf[q] = pk2(acc[1][rt][8 * m + 2 * q], acc[1][rt][8 * m + 2 * q + 1]);
                d = MFMA32(pf[rt][m], vf, d);
            }
        f32x4 k0, k1;
        if (rh) {
            st<f32x4>(mw, 0, d.s0123); st<f32x4>(mw, 1024, d.s4567); k0 = d.s89ab; k1 = d.scdef;
        } else {
            st<f32x4>(mw, 0, d.s89ab); st<f32x4>(mw, 1024, d.scdef); k0 = d.s0123; k1 = d.s4567;
        }
        bar();
        k0 += ld<f32x4>(mr, 0); k1 += ld<f32x4>(mr, 1024);
        st128(rws, po, (u32x4){pk2(k0[0], k0[1]), tag, pk2(k0[2], k0[3]), tag}, 1);
        st128(rws, po + 128, (u32x4){pk2(k1[0], k1[1]), tag, pk2(k1[2], k1[3]), tag}, 1);
        SB();
        WO4(2, 0) WO2(3, 0, 1) WO2(4, 2, 3)
        vmw<HPOS ? 15 : 14>();
        BAR();
        WRD(0, 1) WRD(1, 2)
    }
    } else {
        const rsrc_t ru = mk_rsrc((const char*)u - 4096, 2048 * 896 + 4096);
#define X_RNL 0
#define X_RNS 0
        const u32 uo = (lane >> 3) * 896 + (lane & 7) * 16 + 4096 + X_RNL * (OOB / 2), u2 = uo + (w < 3 ? 128 - 2048 : OOB), us = (b - 192) * (32 * 896) + w * 256;
        const u32 ub = A_W + w * 8192;
        u32 p2a, p2b;
#define P2() {     \
            const u32 tx = (idx & 7) * 256 + tid, rq = tx * 43691u >> 19, l = tx - rq * 12; \
            const u32 row = rq < 96 ? rq + 96 + (rq >> 5) * 96 : rq + 288; \
            const u32 ow = row * 2688 + (l >> 2) * 896 + (l & 3) * 128, ox = tx & 3 ? OOB : tx * 224; \
            asm volatile("buffer_load_dword %0, %2, %4, %6 offen\n\tbuffer_load_dword %1, %3, %5, %7 offen" : "=&v"(p2a), "=&v"(p2b) \
                         : "v"(ow), "v"(ox), "s"(mk_rsrc(wqkv, 448 * 2688)), "s"(mk_rsrc(x, 2048 * 896)), "s"((b & 1) * 384), "s"(((b >> 1) & 3) * 458752 + 768) : "memory"); \
        }
        M0(ub, 0); NOP();
        dmao<0>(ru, uo, us); dmao<1024>(ru, uo, us + 7168 - 1024); dmao<2048>(ru, u2, us); dmao<3072>(ru, u2, us + 7168 - 1024);
        M0(ub, 4096); NOP();
        dmao<0>(ru, uo, us + 14336); dmao<1024>(ru, uo, us + 21504 - 1024); dmao<2048>(ru, u2, us + 14336); dmao<3072>(ru, u2, us + 21504 - 1024);
        P2()
        LATE0()
        u32 eb0 = sld(ws + WS_EPOCH + 8 * w), eb1 = sld(ws + WS_EPOCH + 8 * w + 4);
        u32 pfd[2] = {0, 0};
        const u32 pw = (((idx & 7) * 4 + w) * 64 + lane) * 128 + 4096;
        AUX1() AUX2() AUX3() TAG()
        const lptr ra = lp(ub + hh * 4096 + (8 * (g & 1) + (l15 >> 2)) * 128 + (l15 & 3) * 8);
        const u32 s0 = X_RNS * (OOB / 2) + WS_UT + (u32)(w * 64 + (l15 >> 1)) * UT_S + (b - 192) * 128 + (l15 & 1) * 64 + g * 16, s1 = s0 + (w < 3 ? 32 * UT_S : OOB);
        const u32 fo = (lane < 2 && 2 * w + lane < 7) ? WS_UF + ((2 * w + lane) * 4 + ((b >> 4) & 3)) * 128 + (b & 15) * 4 : OOB, ft = mk_tag(lane ? eb1 : eb0);
        u32x4 d[8];
        vmw<2>();
        asm volatile("" :: "v"(epf));
#pragma unroll
        for (int q = 0; q < 8; ++q) d[q] = ldw2(ra, (q >> 2) * 2048 + (q & 3) * 32);
#pragma unroll
        for (int q = 0; q < 8; ++q) st128(rws, (q & 4 ? s1 : s0) + (q & 3) * 8 * UT_S, d[q], 1);
        SB();
        vmw<0>();
        asm volatile("" :: "v"(p2a), "v"(p2b));
        __builtin_amdgcn_raw_buffer_store_b32(ft, rws, fo, 0, 16);
        asm volatile("buffer_load_dword %0, %2, %3, 0 offen\n\tbuffer_load_dword %1, %2, %3, %4 offen" : "=&v"(pfd[0]), "=&v"(pfd[1]) : "v"(pw), "s"(ro), "s"(262144) : "memory");
        u32 cpf = min(tid * 128u, CODE_PF - 128u);
#define PFT() asm volatile("" :: "v"(pfd[0]), "v"(pfd[1]), "v"(cpf))
        WO4(0, 1) WO4(1, 2) WO4(2, 0)
        BAR();
        WO2(3, 0, 1) WO2(4, 2, 3)
        vmw<12>();
        PFT();
        BAR();
        WRD(0, 1) WRD(1, 2)
    }
    WQ()
    const u32 qs = WS_PQ + r32 * 32768 + w * 8192, ks = WS_KVG + w * 8192, fs = WS_QF + (rg * 4 + w) * 128;
#pragma unroll
    for (int k = 0; k < 8; ++k) { asm volatile("" : "=v"(pq[k])); asm volatile("" : "=v"(kv[k])); }
    asm volatile("" : "=v"(hv), "=v"(fl), "=v"(fq));
#pragma unroll
    for (int s = 0; s < 8; ++s) PIN(wr[s]);
    if (jj != 2)
    {
    BAR();
    WO4(5, 1) WO4(6, 2)
    }
    gl1<0>(fl, rws, vo & 112, WS_CFLAG);
    gl1<0>(fq, rws, opq(0), fs);
    gl1<0>(hv, rws, H2V, H2S);
#undef PHI
    {
        lptr wpc[4];
#pragma unroll
        for (int r = 0; r < 4; ++r) { wpc[r] = lp(A_W + wl + hh * 8192 + ((w + r) & 3) * 2048); PIN(wpc[r]); }
        if (jj == 2) {
            vmw<3>();
            BAR();
            WRD(4, 0)
#pragma unroll
            for (int r = 0; r < 4; ++r) { wr[20 + r] = ldw(lp((u32)(size_t)wpa[r] ^ 128), 0); wr[24 + r] = ldw(wpc[r], 0); }
        } else
        {
        vmw<11>();
        BAR();
        WRD(2, 0)
#pragma unroll
        for (int r = 0; r < 4; ++r) { wr[12 + r] = ldw(wpc[r], 0); wr[16 + r] = ldw(wpc[r], 16384); }
        vmw<3>();
        BAR();
        WRD(5, 1) WRD(6, 2)
        }
    }
    if (jj == 3) {
        const u32 sg = (b - 192) * 4 + w;
        bar();
        flags_ok<1>(rws, vo & 112, WS_CFLAG, tag, fl);
#define RH1 (WS_H1 + (b - 192) * 128)
        if (w == 0)
        gl1<0>(hv, rws, vo & 112, RH1);
        u32x4 sel = {0u, 0u, 0u, 0u};
        sel[0] = l15 == 0 ? 0x00003f80u : (l15 == 1 ? 0x3f800000u : 0u);
        sel[2] = l15 == 2 ? 0x00003f80u : (l15 == 3 ? 0x3f800000u : 0u);
        u32 ko = lane < 16 ? WS_KVG + sg * 128 + (lane & 7) * 16 + (lane & 8) : OOB;
        u32 go = lane < 4 ? WS_H2 + ((sg & 63) * 4 + lane) * 128 + (sg >> 6) * 8 : OOB;
        PIN(ko); PIN(sel); PIN(go);
        int sp;
        u32 t1;
        if (w == 0) { t1 = hint_wait<1, R_SLP>(rws, vo & 112, RH1, tag, hv, sp); st<u32>(lp(81916), 0, t1); }
        bar();
        t1 = __builtin_amdgcn_readfirstlane(ld<u32>(lp(81916), 0));
        __builtin_amdgcn_raw_buffer_store_b64((u32x2){t1, tag}, rws, go, 0, 16);
        until(t1 + A_D1);
        u32x4 pv[4];
#pragma unroll
        for (int k = 0; k < 4; ++k) asm volatile("" : "=v"(pv[k]));
        gload<4>(rws, vo, WS_PART + sg * PT_S, 4096, pv);
        gl1<0>(fq, rws, opq(0), fs);
        gl1<0>(hv, rws, H2V, H2S);
        sp = swait<4, 2, 1>(rws, vo, WS_PART + sg * PT_S, tag, pv);
        f32x4 s = {0.f, 0.f, 0.f, 0.f}, s2 = s;
#pragma unroll
        for (int k = 0; k < 2; ++k) { s = MFMA16(sel, pv[2 * k], s); s2 = MFMA16(sel, pv[2 * k + 1], s2); }
        s += s2;
        __builtin_amdgcn_raw_buffer_store_b64((u32x2){nos(pk2(s[0], s[1])), nos(pk2(s[2], s[3]))}, rws, ko, 0, 16);
    }
    {
        if (jj == 3) vmw<1>(); else vmw<0>();
        int sp = flags_ok<0, Q_SLP>(rws, opq(0), fs, tag, fq);
        gload<8>(rws, vo, qs, 4096, pq);
    }
    flags_ok<0>(rws, vo & 112, WS_CFLAG, tag, fl);
    {
        int sp;
        const u32 t2 = hint_wait<0>(rws, H2V, H2S, tag, hv, sp);
        until(t2 + A_D2);
    }
    gload<8>(rws, vo + X_NOKV * OOB, ks, 4096, kv);
    if (b == 0 && w < 2) gl1<0>(fl, rws, lane * 128, WS_QF + w * 8192);
#pragma unroll
    for (int s = 0; s < 28; ++s) PIN(wr[s]);
    BAR();
    {
        int sp = swait<8, 0, 0>(rws, vo, ks, 0, kv);
        tie(pq[0], pq[1], pq[2], pq[3]); tie(pq[4], pq[5], pq[6], pq[7]);
    }
    if (b == 0 && w < 2) flags_ok<1>(rws, lane * 128, WS_QF + w * 8192, tag, fl);
    {
        f32x16 oc = splat16(0.f), oc2 = oc, nm[4];
        float ov[56];
        const lptr ow = lp(xb + lane * 16);
#define NUM(h) { nm[h] = MFMA32(kv[2 * (h)], pq[2 * (h)], splat16(0.f)); nm[h] = MFMA32(kv[2 * (h) + 1], pq[2 * (h) + 1], nm[h]); PIN(nm[h]); }
        NUM(0) NUM(1)
        SB();
#pragma unroll
        for (int h = 0; h < 4; ++h) {
            float r = __builtin_amdgcn_rcpf(nm[h][14] + 1e-6f);
            if (h + 2 < 4) NUM(h + 2)
#pragma unroll
            for (int i = 0; i < 14; ++i) { ov[14 * h + i] = nm[h][i] * r; PIN(ov[14 * h + i]); }
#pragma unroll
            for (int f = 0; f < 7; ++f)
                if (8 * f + 7 < 14 * h + 14 && 8 * f + 7 >= 14 * h) {
                    u32x4 of = {pk2(ov[8 * f], ov[8 * f + 1]), pk2(ov[8 * f + 2], ov[8 * f + 3]), pk2(ov[8 * f + 4], ov[8 * f + 5]), pk2(ov[8 * f + 6], ov[8 * f + 7])};
                    st<u32x4>(ow, (f >> 2) * 16384 + (f & 3) * 1024, of);
                    oc = MFMA32(wr[4 * f], of, oc);
                    PIN(oc);
                }
            SB();
        }
#undef NUM
        bar();
        if (act) {
            u32x4 of[21];
#pragma unroll
            for (int r = 1; r < 4; ++r) {
                const lptr orp = lp(((w + r) & 3) * 4096 + lane * 16);
#pragma unroll
                for (int f = 0; f < 7; ++f) of[7 * r - 7 + f] = ld<u32x4>(orp, (f >> 2) * 16384 + (f & 3) * 1024);
            }
#pragma unroll
            for (int s = 0; s < 21; ++s) { if (s & 1) oc = MFMA32(wr[4 * (s % 7) + 1 + s / 7], of[s], oc); else oc2 = MFMA32(wr[4 * (s % 7) + 1 + s / 7], of[s], oc2); }
#pragma unroll
            for (int i = 0; i < 16; ++i) { oc[i] += oc2[i]; PIN(oc[i]); }
            const rsrc_t rout = mk_rsrc(out, 2048 * 896 * 2);
            const u32 oo = (u32)(r32 * 32 + l15) * 1792 + wso + g * 16;
            u32x4 o0, o1;
#pragma unroll
            for (int i = 0; i < 4; ++i) {
                auto sw = __builtin_amdgcn_permlane16_swap(pk2(oc[2 * i], oc[2 * i + 1]), pk2(oc[8 + 2 * i], oc[9 + 2 * i]), false, false);
                o0[i] = sw[0]; o1[i] = sw[1];
            }
            st128(rout, oo, o0, 1);
            st128(rout, oo + 16 * 1792, o1, 1);
        }
        if (b == 0 && w == 0) __builtin_amdgcn_raw_buffer_store_b32(tag, rws, lane ? OOB : 0, WS_EPOCH + 32, 16);
    }
}
#undef jj
#undef cj
#undef idx
#undef t
#undef R
#undef rh
#undef hd
#undef rg
#undef r32
#undef act
#undef wso

__global__ __launch_bounds__(256, 2) void fused_kernel(const u16* x, const u16* wqkv, const u16* wo, const u16* u,
        const u16* lar, const u16* aim, const u16* cre, const u16* cim, const u16* ldt, const u16* dsk, u16* out, char* ws) {
    __shared__ __attribute__((aligned(16))) char smem[81920];
    asm volatile("" :: "v"((u32)(size_t)(LDSAS char*)smem) : "memory");
    const int b = blockIdx.x;
    u64 pc;
    asm volatile("s_getpc_b64 %0" : "=s"(pc));
    u32 epf = min((b & 56) * 512u + threadIdx.x * 16u, X_EPF - 128u);
    if ((b >> 6) == X_EPB)
    asm volatile("global_load_dword %0, %0, %1" : "+v"(epf) : "s"(pc) : "memory");
    if (__builtin_expect(b >= 256, 0)) {
        role_b((b - SPM) & 255, lar, aim, cre, cim, ldt, dsk, out, ws);
    } else {
        role_a(b, pc, epf, x, wqkv, wo, u, out, ws);
    }
}

extern "C" void kernel_launch(void* const* d_in, const int* in_sizes, int n_in, void* d_out, int out_size,
                              void* d_ws, size_t ws_size, hipStream_t stream) {
    fused_kernel<<<512, 256, 0, stream>>>((const u16*)d_in[0], (const u16*)d_in[1], (const u16*)d_in[2], (const u16*)d_in[3],
        (const u16*)d_in[4], (const u16*)d_in[5], (const u16*)d_in[6], (const u16*)d_in[7], (const u16*)d_in[8],
        (const u16*)d_in[9], (u16*)d_out, (char*)d_ws + WS_BASE);
}
```
